```python
import torch

def _softmax(x, axis=-1):
    return torch.softmax(x, dim=axis)
def _gelu(x):
    return torch.nn.functional.gelu(x, approximate="tanh")
def _silu(x):
    return torch.nn.functional.silu(x)
def _sigmoid(x):
    return torch.sigmoid(x)
def _layernorm(x, axis=-1, eps=1e-5):
    m = x.mean(dim=axis, keepdim=True)
    v = x.var(dim=axis, keepdim=True, unbiased=False)
    return (x - m) / torch.sqrt(v + eps)
def _scatter_add(tgt, idx, src, axis=0):
    return tgt.clone().index_add_(axis, idx, src)
def _segment_sum(src, seg, n):
    out = torch.zeros((n,) + src.shape[1:], dtype=src.dtype)
    return out.index_add_(0, seg, src)
def _pad(x, pw):
    flat = tuple(v for pair in reversed(pw) for v in pair)
    return torch.nn.functional.pad(x, flat)

kernel_name = 'standalone__weight_only_int_quip__decode'

def setup_inputs():

    _w = (lambda a: (lambda t: t.to(torch.bfloat16) if t.is_floating_point() else t)(torch.as_tensor(a)))
    B, S, D, F = 4, 1, 576, 4608
    G = 32 if 32 > 0 else D
    D = (D // G) * G
    ng = D // G
    bits = 4 if 4 > 0 else 4
    qmax = 1 << bits
    qw = torch.randint(0, qmax, (D, F)).to(torch.int32)
    U, _ = (lambda *_a, **_k: torch.linalg.qr(*[torch.as_tensor(_x).float() for _x in _a], **_k))((lambda *s: torch.randn(*s, dtype=torch.bfloat16) * 0.05)(D, D))
    V, _ = (lambda *_a, **_k: torch.linalg.qr(*[torch.as_tensor(_x).float() for _x in _a], **_k))((lambda *s: torch.randn(*s, dtype=torch.bfloat16) * 0.05)(F, F))
    return {
        "x": _w((lambda *s: torch.randn(*s, dtype=torch.bfloat16) * 0.05)(B, S, D)),
        "qw": _w(qw),
        "scales": _w(torch.abs((lambda *s: torch.randn(*s, dtype=torch.bfloat16) * 0.05)(ng, F)) * 0.02 + 0.005),
        "zeros": _w(torch.full((ng, F), qmax / 2.0, dtype=float)),
        "U": _w(U.to(float)),
        "V": _w(V.to(float)),
    }

def reference(x, qw, scales, zeros, U, V):
    B, S, D = x.shape
    ng, F = scales.shape
    G = D // ng
    s = (lambda x, n, axis: torch.repeat_interleave(x, n, dim=axis))(scales, G, axis=0)
    z = (lambda x, n, axis: torch.repeat_interleave(x, n, dim=axis))(zeros, G, axis=0)
    wq = ((lambda x, dt: x.to(dt))(qw, x.dtype) - z) * s
    xr = torch.einsum("bsd,de->bse", x, U)
    h  = torch.einsum("bse,ef->bsf", xr, wq)
    y  = torch.einsum("bsf,fg->bsg", h, V)
    return y
```

```cpp
#include <hip/hip_runtime.h>
#include <hip/hip_bf16.h>

constexpr int B = 4, D = 576, F = 4608, G = 32, NG = 18;

__device__ __forceinline__ float bf2f(unsigned short b) { return __builtin_bit_cast(float, (unsigned)b << 16); }
__device__ __forceinline__ unsigned short f2bf(float f) {
  __hip_bfloat16 h = __float2bfloat16(f);
  return __builtin_bit_cast(unsigned short, h);
}
__device__ __forceinline__ float rbf(float f) { return bf2f(f2bf(f)); }

__global__ void k1(const unsigned short* x, const unsigned short* U, float* xr) {
  int e = blockIdx.x * 64 + threadIdx.x;
  float acc[B] = {0, 0, 0, 0};
  for (int d = 0; d < D; ++d) {
    float u = bf2f(U[d * D + e]);
    for (int b = 0; b < B; ++b) acc[b] += bf2f(x[b * D + d]) * u;
  }
  for (int b = 0; b < B; ++b) xr[b * D + e] = rbf(acc[b]);
}
__global__ void k2(const float* xr, const int* qw, const unsigned short* sc, const unsigned short* zr, float* h) {
  int f = blockIdx.x * 64 + threadIdx.x;
  float acc[B] = {0, 0, 0, 0};
  for (int e = 0; e < D; ++e) {
    float s = bf2f(sc[(e / G) * F + f]), z = bf2f(zr[(e / G) * F + f]);
    float w = rbf(rbf(rbf((float)qw[e * F + f]) - z) * s);
    for (int b = 0; b < B; ++b) acc[b] += xr[b * D + e] * w;
  }
  for (int b = 0; b < B; ++b) h[b * F + f] = rbf(acc[b]);
}
constexpr int SPL = 32;
__global__ void k3(const float* h, const unsigned short* V, float* part) {
  int g = blockIdx.x * 64 + threadIdx.x;
  int s = blockIdx.y;
  float acc[B] = {0, 0, 0, 0};
  for (int f = s * (F / SPL); f < (s + 1) * (F / SPL); ++f) {
    float v = bf2f(V[(size_t)f * F + g]);
    for (int b = 0; b < B; ++b) acc[b] += h[b * F + f] * v;
  }
  for (int b = 0; b < B; ++b) part[(s * B + b) * F + g] = acc[b];
}
__global__ void k4(const float* part, unsigned short* out) {
  int i = blockIdx.x * 64 + threadIdx.x;
  float a = 0;
  for (int s = 0; s < SPL; ++s) a += part[s * B * F + i];
  out[i] = f2bf(a);
}

extern "C" void kernel_launch(void* const* d_in, const int* in_sizes, int n_in,
                              void* d_out, int out_size, void* d_ws, size_t ws_size,
                              hipStream_t stream) {
  const unsigned short* x = (const unsigned short*)d_in[0];
  const int* qw = (const int*)d_in[1];
  const unsigned short* sc = (const unsigned short*)d_in[2];
  const unsigned short* zr = (const unsigned short*)d_in[3];
  const unsigned short* U = (const unsigned short*)d_in[4];
  const unsigned short* V = (const unsigned short*)d_in[5];
  float* xr = (float*)d_ws;
  float* h = xr + 4096;
  float* part = h + B * F;
  k1<<<D / 64, 64, 0, stream>>>(x, U, xr);
  k2<<<F / 64, 64, 0, stream>>>(xr, qw, sc, zr, h);
  k3<<<dim3(F / 64, SPL), 64, 0, stream>>>(h, V, part);
  k4<<<B * F / 64, 64, 0, stream>>>(part, (unsigned short*)d_out);
}
```

```cpp
#include <hip/hip_runtime.h>
#include <hip/hip_bf16.h>

namespace qk {

typedef unsigned u32;
typedef unsigned long long u64;
typedef u32 v4u __attribute__((ext_vector_type(4)));
typedef u32 v2u __attribute__((ext_vector_type(2)));
typedef float v4f __attribute__((ext_vector_type(4)));
typedef float v2f __attribute__((ext_vector_type(2)));
typedef short v4s __attribute__((ext_vector_type(4)));
typedef __bf16 v8bf __attribute__((ext_vector_type(8)));
typedef __bf16 v2bf __attribute__((ext_vector_type(2)));
typedef __amdgpu_buffer_rsrc_t rsrc_t;

constexpr int D = 576, F = 4608;
constexpr int VP = F * 2;
constexpr int QP = F * 4;
constexpr int UP = D * 2;

#define KN 5
#define KA 4
#define KF 3
#define PN 32
#define PF 31
#define NEARF 31
#define AUXV 0
#define AUXQ 0
constexpr int SC1 = 16;

constexpr u32 WS_E = 0;
constexpr u32 WS_ACK = 4096;
constexpr u32 WS_XR = 8192;
constexpr u32 WS_H = 32768;
constexpr u32 WS_P = 131072;
constexpr u32 WS_BYTES = WS_P + 72 * 12288;
constexpr u32 WS_DBG = 2u << 20;

constexpr int SPIN_LDS = 1 << 22;
constexpr int SPIN_MEM = 1 << 18;

struct __attribute__((aligned(16))) Lds {
  u64 hflag;
  u32 nearEnd;
  u32 pad0;
  u32 cnt[4];
  float redC[4][256];
  float redV[8][512];
  v2u himg[8][256];
};

#define STAMP(i)
#define STDECL
#define STARG
#define STPASS

template <int N> __device__ __forceinline__ void waitvm() { asm volatile("s_waitcnt vmcnt(%0)" :: "n"(N) : "memory"); }
__device__ __forceinline__ void waitlgkm() { asm volatile("s_waitcnt lgkmcnt(0)" ::: "memory"); }
__device__ __forceinline__ void cbar() { asm volatile("" ::: "memory"); }
__device__ __forceinline__ void pin() { __builtin_amdgcn_sched_barrier(0); }
__device__ __forceinline__ void wgbar() { cbar(); __builtin_amdgcn_s_barrier(); cbar(); }
__device__ __forceinline__ rsrc_t mkrs(const void* p, u32 bytes) { return __builtin_amdgcn_make_buffer_rsrc((void*)p, 0, (int)bytes, 0x00020000); }
__device__ __forceinline__ v4u ldg4(rsrc_t rs, u32 off) { return __builtin_amdgcn_raw_buffer_load_b128(rs, off, 0, SC1); }
__device__ __forceinline__ void stg4(rsrc_t rs, u32 off, v4u v) { __builtin_amdgcn_raw_buffer_store_b128(v, rs, off, 0, SC1); }
__device__ __forceinline__ u32 pkbf(float a, float b) {
  v2f t = {a, b};
  return __builtin_bit_cast(u32, __builtin_convertvector(t, v2bf));
}
__device__ __forceinline__ float bflo(u32 w) { return __builtin_bit_cast(float, w << 16); }
__device__ __forceinline__ float bfhi(u32 w) { return __builtin_bit_cast(float, w & 0xffff0000u); }
__device__ __forceinline__ u32 perm(u32 w, u32 sel) { return __builtin_amdgcn_perm(w, w, sel); }
__device__ __forceinline__ bool tagok(v4u g, u32 tag) { return g.y == tag && g.w == tag; }
__device__ __forceinline__ u32 fbits(float f) { return __builtin_bit_cast(u32, f); }
__device__ __forceinline__ float bitsf(u32 u) { return __builtin_bit_cast(float, u); }

struct Sel { u32 aLo, bLo, aHi, bHi; };
__device__ __forceinline__ Sel mksel(int lane) {
  const int s = (lane & 15) >> 2;
  Sel r;
  r.aLo = s == 0 ? 0x0c0c0100u : s == 1 ? 0x01000c0cu : 0x0c0c0c0cu;
  r.bLo = s == 2 ? 0x0c0c0100u : s == 3 ? 0x01000c0cu : 0x0c0c0c0cu;
  r.aHi = s == 0 ? 0x0c0c0302u : s == 1 ? 0x03020c0cu : 0x0c0c0c0cu;
  r.bHi = s == 2 ? 0x0c0c0302u : s == 3 ? 0x03020c0cu : 0x0c0c0c0cu;
  return r;
}
__device__ __forceinline__ v4f mfma16(v2u a, v2u b, v4f c) {
  return __builtin_amdgcn_mfma_f32_16x16x16bf16_1k(__builtin_bit_cast(v4s, a), __builtin_bit_cast(v4s, b), c, 0, 0, 0);
}
__device__ __forceinline__ v4f mfma32(v4u a, v4u b, v4f c) {
  return __builtin_amdgcn_mfma_f32_16x16x32_bf16(__builtin_bit_cast(v8bf, a), __builtin_bit_cast(v8bf, b), c, 0, 0, 0);
}

__device__ __forceinline__ bool arrive_last(u32* cnt, u32 n, int lane) {
  waitlgkm();
  u32 old = 0;
  if (lane == 0) old = __hip_atomic_fetch_add(cnt, 1u, __ATOMIC_RELAXED, __HIP_MEMORY_SCOPE_WORKGROUP);
  old = __builtin_amdgcn_readfirstlane(old);
  cbar();
  return old == n - 1;
}
__device__ __forceinline__ u32 wait_hflag(Lds& L) {
  u64 f = 0;
#pragma nounroll
  for (int it = 0; it < SPIN_LDS; ++it) {
    f = __hip_atomic_load(&L.hflag, __ATOMIC_RELAXED, __HIP_MEMORY_SCOPE_WORKGROUP);
    if (__builtin_amdgcn_readfirstlane((u32)(f >> 32))) break;
    __builtin_amdgcn_s_sleep(1);
  }
  cbar();
  return __builtin_amdgcn_readfirstlane((u32)f);
}
__device__ __forceinline__ void watch_h(Lds& L, rsrc_t rw, u32 tag, int strip0, int nstrip, int lane) {
  const u32 off = WS_H + (strip0 + lane) * 512;
  bool ok = lane != 0;
#pragma nounroll
  for (int it = 0; it < SPIN_MEM; ++it) {
    cbar();
    if (!ok) ok = tagok(ldg4(rw, off), tag);
    if (__all(ok)) break;
    __builtin_amdgcn_s_sleep(4);
  }
  ok = lane == 0 || lane >= nstrip;
#pragma nounroll
  for (int it = 0; it < SPIN_MEM; ++it) {
    cbar();
    if (!ok) ok = tagok(ldg4(rw, off), tag);
    if (__all(ok)) break;
    __builtin_amdgcn_s_sleep(1);
  }
  cbar();
  if (lane == 0) __hip_atomic_store(&L.hflag, (u64)tag | (1ull << 32), __ATOMIC_RELAXED, __HIP_MEMORY_SCOPE_WORKGROUP);
}

template <int N>
__device__ __forceinline__ void fix_granules(v4u (&g)[N], rsrc_t rw, u32 off, u32 stride, u32 tag) {
  bool ok = true;
#pragma unroll
  for (int j = 0; j < N; ++j) ok = ok && tagok(g[j], tag);
  if (__builtin_expect(!__all(ok), 0)) {
#pragma nounroll
    for (int it = 0; it < SPIN_MEM; ++it) {
      cbar();
      __builtin_amdgcn_s_sleep(1);
      ok = true;
#pragma unroll
      for (int j = 0; j < N; ++j) { g[j] = ldg4(rw, off + j * stride); }
#pragma unroll
      for (int j = 0; j < N; ++j) ok = ok && tagok(g[j], tag);
      if (__all(ok)) break;
    }
  }
}

template <int K, int P>
__device__ __forceinline__ void v256(Lds& L, rsrc_t rv, rsrc_t rw, int team, int row0, int w, int lane, bool early,
                                     int grp, int gw0, int gn, int member STARG) {
  constexpr int NL = 32;
  const int q = lane >> 4, n = lane & 15, b = lane & 3;
  const u32 voff = (row0 + 4 * q) * VP + team * 256 + n * 16;
  v4u v[NL];
  auto issue = [&](int i) { v[i] = __builtin_amdgcn_raw_buffer_load_b128(rv, voff, (16 * (i >> 2) + (i & 3)) * VP, AUXV); };
  if (!early) wgbar();
#pragma unroll
  for (int i = 0; i < K; ++i) { issue(i); pin(); }
  if (early) wgbar();
  const Sel sel = mksel(lane);
#pragma unroll
  for (int i = K; i < P; ++i) { waitvm<K - 1>(); issue(i); pin(); }
  STAMP(1);
  const u32 tag = wait_hflag(L);
  STAMP(2);
  v4u g[2];
  const u32 hoff = WS_H + row0 * 16 + lane * 16;
  g[0] = ldg4(rw, hoff); g[1] = ldg4(rw, hoff + 1024);
  pin();
  constexpr int P2 = P + K < NL ? P + K : NL;
#pragma unroll
  for (int i = P; i < P2; ++i) { waitvm<K - 1>(); issue(i); pin(); }
  fix_granules<2>(g, rw, hoff, 1024, tag);
  STAMP(3);
  L.himg[w][lane] = v2u{g[0].x, g[0].z};
  L.himg[w][64 + lane] = v2u{g[1].x, g[1].z};
  waitlgkm();
  v2u hq[8];
#pragma unroll
  for (int p = 0; p < 8; ++p) hq[p] = L.himg[w][(4 * p + q) * 4 + b];
  v4f acc0 = {0, 0, 0, 0}, acc1 = {0, 0, 0, 0};
  auto work = [&](int j) {
    const u32 hw = (j & 2) ? hq[j >> 2].y : hq[j >> 2].x;
    const v2u a = (j & 1) ? v2u{perm(hw, sel.aHi), perm(hw, sel.bHi)} : v2u{perm(hw, sel.aLo), perm(hw, sel.bLo)};
    acc0 = mfma16(a, v2u{v[j].x, v[j].y}, acc0);
    acc1 = mfma16(a, v2u{v[j].z, v[j].w}, acc1);
  };
  constexpr int R = NL - P2;
  constexpr int C = R > 0 ? (NL - K + R - 1) / R : 0;
  int done = 0;
#pragma unroll
  for (int i = P2; i < NL; ++i) {
    waitvm<K - 1>(); issue(i); pin();
    const int lim = (i - P2 + 1) * C < i - K + 1 ? (i - P2 + 1) * C : i - K + 1;
#pragma unroll
    for (int j = 0; j < NL; ++j) if (j >= done && j < lim) work(j);
    done = lim;
    pin();
  }
  STAMP(4);
#pragma unroll
  for (int j = 0; j < NL; ++j) if (j >= done) work(j);
  STAMP(5);
  {
    float* dst = &L.redV[w][8 * n + q];
#pragma unroll
    for (int r = 0; r < 4; ++r) { dst[r * 128] = acc0[r]; dst[r * 128 + 4] = acc1[r]; }
  }
  if (arrive_last(&L.cnt[1 + grp], gn, lane)) {
    const int Q = lane >> 2;
    v4f s0 = {0, 0, 0, 0}, s1 = {0, 0, 0, 0};
    for (int k = 0; k < gn; ++k) {
      const float* src = &L.redV[gw0 + k][b * 128 + 4 * Q];
      s0 += *(const v4f*)src;
      s1 += *(const v4f*)(src + 64);
    }
    const u32 po = WS_P + (team * 2) * 12288 + member * 2048 + lane * 16;
    stg4(rw, po, v4u{fbits(s0[0]), tag, fbits(s0[1]), tag});
    stg4(rw, po + 1024, v4u{fbits(s0[2]), tag, fbits(s0[3]), tag});
    stg4(rw, po + 12288, v4u{fbits(s1[0]), tag, fbits(s1[1]), tag});
    stg4(rw, po + 12288 + 1024, v4u{fbits(s1[2]), tag, fbits(s1[3]), tag});
    STAMP(6);
  }
}

__device__ __forceinline__ void finish(Lds& L, unsigned short* __restrict__ out, int col0, int lane) {
  const int Q = lane >> 2, b = lane & 3;
  v4f s = {0, 0, 0, 0};
#pragma unroll
  for (int k = 0; k < 8; ++k) s += *(const v4f*)&L.redV[k][b * 64 + 4 * Q];
  *(v2u*)(out + b * F + col0 + 4 * Q) = v2u{pkbf(s[0], s[1]), pkbf(s[2], s[3])};
}

template <int K, int P>
__device__ __forceinline__ void v128(Lds& L, rsrc_t rv, rsrc_t rw, unsigned short* __restrict__ out, int col0, int row0, int w, int lane STARG) {
  constexpr int NL = 32;
  const int q = lane >> 4, n = lane & 15, b = lane & 3;
  const u32 voff = (row0 + 4 * q) * VP + col0 * 2 + n * 8;
  v2u v[2 * NL];
  auto issue = [&](int i) {
    const int r = 16 * (i >> 1) + 2 * (i & 1);
    v[2 * i] = __builtin_amdgcn_raw_buffer_load_b64(rv, voff, r * VP, AUXV);
    v[2 * i + 1] = __builtin_amdgcn_raw_buffer_load_b64(rv, voff, (r + 1) * VP, AUXV);
  };
#pragma unroll
  for (int i = 0; i < K; ++i) { issue(i); pin(); }
  wgbar();
  const Sel sel = mksel(lane);
#pragma unroll
  for (int i = K; i < P; ++i) { waitvm<2 * K - 2>(); issue(i); pin(); }
  STAMP(1);
  const u32 tag = wait_hflag(L);
  STAMP(2);
  v4u g[4];
  const u32 hoff = WS_H + row0 * 16 + lane * 16;
#pragma unroll
  for (int j = 0; j < 4; ++j) g[j] = ldg4(rw, hoff + j * 1024);
  pin();
  constexpr int P2 = P + K < NL ? P + K : NL;
  auto near = [&](int i) { if (i == NEARF && lane == 0) __hip_atomic_store(&L.nearEnd, 1u, __ATOMIC_RELAXED, __HIP_MEMORY_SCOPE_WORKGROUP); };
  static_assert(NEARF >= P && NEARF < NL, "nearf");
#pragma unroll
  for (int i = P; i < P2; ++i) { waitvm<2 * K - 2>(); issue(i); near(i); pin(); }
  fix_granules<4>(g, rw, hoff, 1024, tag);
  STAMP(3);
#pragma unroll
  for (int j = 0; j < 4; ++j) L.himg[w][64 * j + lane] = v2u{g[j].x, g[j].z};
  waitlgkm();
  v2u hq[16];
#pragma unroll
  for (int p = 0; p < 16; ++p) hq[p] = L.himg[w][(4 * p + q) * 4 + b];
  v4f acc = {0, 0, 0, 0};
  auto work = [&](int j) {
    const u32 hw = (j & 1) ? hq[j >> 1].y : hq[j >> 1].x;
    const v4u a = {perm(hw, sel.aLo), perm(hw, sel.bLo), perm(hw, sel.aHi), perm(hw, sel.bHi)};
    acc = mfma32(a, v4u{v[2 * j].x, v[2 * j].y, v[2 * j + 1].x, v[2 * j + 1].y}, acc);
  };
  constexpr int R = NL - P2;
  constexpr int C = R > 0 ? (NL - K + R - 1) / R : 0;
  int done = 0;
#pragma unroll
  for (int i = P2; i < NL; ++i) {
    waitvm<2 * K - 2>(); issue(i); near(i); pin();
    const int lim = (i - P2 + 1) * C < i - K + 1 ? (i - P2 + 1) * C : i - K + 1;
#pragma unroll
    for (int j = 0; j < NL; ++j) if (j >= done && j < lim) work(j);
    done = lim;
    pin();
  }
  STAMP(4);
#pragma unroll
  for (int j = 0; j < NL; ++j) if (j >= done) work(j);
  STAMP(5);
  {
    float* dst = &L.redV[w][4 * n + q];
#pragma unroll
    for (int r = 0; r < 4; ++r) dst[r * 64] = acc[r];
  }
  if (arrive_last(&L.cnt[1], 8, lane)) { finish(L, out, col0, lane); STAMP(6); }
}

__global__ void __launch_bounds__(512, 2)
quip_kernel(const unsigned short* __restrict__ x, const int* __restrict__ qw, const unsigned short* __restrict__ sc,
            const unsigned short* __restrict__ zr, const unsigned short* __restrict__ U, const unsigned short* __restrict__ V,
            unsigned short* __restrict__ out, unsigned char* __restrict__ ws) {
  __shared__ Lds L;
  STDECL;
  STAMP(0);
  const int tid = threadIdx.x, lane = tid & 63;
  const int w = __builtin_amdgcn_readfirstlane(tid >> 6);
  const int wg = blockIdx.x, xc = wg & 7, j = wg >> 3;
  const rsrc_t rw = mkrs(ws, WS_BYTES);
  const rsrc_t rv = mkrs(V, F * VP);

  int role, id;
  if (j >= 23) { role = 4; id = (j - 23) * 8 + xc; }
  else if (j >= 5) { role = 1; id = (j - 5) * 8 + xc; }
  else if (j < 3 && xc < 3) { role = 0; id = j * 3 + xc; }
  else {
    const int r = j < 3 ? j * 5 + (xc - 3) : 15 + (j - 3) * 8 + xc;
    if (r < 27) { role = 2; id = 9 + r; } else if (r < 30) { role = 3; id = r - 27; } else { role = 5; id = 0; }
  }

  if (w == 0 && lane < 8) ((u32*)&L)[lane] = 0;

  if (role <= 1) {
    if (w >= 4) {
      const int team = role ? id >> 2 : id;
      const int slot = role ? 4 * (id & 3) + (w - 4) : 16 + (w - 4);
      v256<KN, PN>(L, rv, rw, team, slot * 128, w, lane, false, 0, 4, 4, role ? id & 3 : 4 STPASS);
    } else {
      __builtin_amdgcn_s_setprio(3);
      const int q = lane >> 4, n = lane & 15, b = lane & 3;
      u32 E = __builtin_amdgcn_raw_buffer_load_b32(rw, WS_E, 0, SC1);
      pin();
      if (role == 0) {
        const rsrc_t rx = mkrs(x, 4 * D * 2), ru = mkrs(U, D * UP);
        v2u X[9], Ut[36];
        const u32 xoff = b * UP + (144 * w + 4 * q) * 2;
        const u32 uoff = (144 * w + 4 * q) * UP + id * 128 + n * 8;
#pragma unroll
        for (int p = 0; p < 9; ++p) {
          X[p] = __builtin_amdgcn_raw_buffer_load_b64(rx, xoff, p * 32, 0);
#pragma unroll
          for (int r = 0; r < 4; ++r) Ut[4 * p + r] = __builtin_amdgcn_raw_buffer_load_b64(ru, uoff, (16 * p + r) * UP, 0);
          pin();
        }
        const u32 tag = __builtin_amdgcn_readfirstlane(E) + 1;
        waitlgkm();
        wgbar();
        STAMP(1);
        if (w == 0 && lane == 0) __builtin_amdgcn_raw_buffer_store_b64(v2u{tag, tag}, rw, WS_ACK + wg * 8, 0, SC1);
        const Sel sel = mksel(lane);
        v4f acc = {0, 0, 0, 0};
#pragma unroll
        for (int c = 0; c < 18; ++c) {
          const u32 hw = (c & 1) ? X[c >> 1].y : X[c >> 1].x;
          const v4u a = {perm(hw, sel.aLo), perm(hw, sel.bLo), perm(hw, sel.aHi), perm(hw, sel.bHi)};
          acc = mfma32(a, v4u{Ut[2 * c].x, Ut[2 * c].y, Ut[2 * c + 1].x, Ut[2 * c + 1].y}, acc);
        }
        STAMP(2);
        float* dst = &L.redC[w][4 * n + q];
#pragma unroll
        for (int r = 0; r < 4; ++r) dst[r * 64] = acc[r];
        if (arrive_last(&L.cnt[0], 4, lane)) {
          const int Q = lane >> 2;
          v4f s = {0, 0, 0, 0};
#pragma unroll
          for (int k = 0; k < 4; ++k) s += *(const v4f*)&L.redC[k][b * 64 + 4 * Q];
          stg4(rw, WS_XR + id * 1024 + lane * 16, v4u{pkbf(s[0], s[1]), tag, pkbf(s[2], s[3]), tag});
          STAMP(3);
          watch_h(L, rw, tag, 64, 16, lane);
          STAMP(4);
        }
      } else {
        const rsrc_t rq = mkrs(qw, D * QP), rsn = mkrs(sc, 18 * VP), rz = mkrs(zr, 18 * VP);
        u32 Z[9], S[9];
        v2u Q[36];
        const u32 soff = id * 64 + n * 4;
        const u32 qoff = (144 * w + 4 * q) * QP + id * 128 + n * 8;
#pragma unroll
        for (int p = 0; p < 9; ++p) {
          const int grp = (144 * w + 16 * p) >> 5;
          Z[p] = __builtin_amdgcn_raw_buffer_load_b32(rz, soff, grp * VP, 0);
          S[p] = __builtin_amdgcn_raw_buffer_load_b32(rsn, soff, grp * VP, 0);
#pragma unroll
          for (int r = 0; r < 4; ++r) Q[4 * p + r] = __builtin_amdgcn_raw_buffer_load_b64(rq, qoff, (16 * p + r) * QP, AUXQ);
          pin();
        }
        const u32 tag = __builtin_amdgcn_readfirstlane(E) + 1;
        waitlgkm();
        wgbar();
        STAMP(1);
        if (w == 0 && lane == 0) __builtin_amdgcn_raw_buffer_store_b64(v2u{tag, tag}, rw, WS_ACK + wg * 8, 0, SC1);
        u32 W[36];
#pragma unroll
        for (int p = 0; p < 9; ++p) {
          const float z0 = bflo(Z[p]), z1 = bfhi(Z[p]), s0 = bflo(S[p]), s1 = bfhi(S[p]);
#pragma unroll
          for (int r = 0; r < 4; ++r) {
            const u32 a = pkbf((float)(int)Q[4 * p + r].x, (float)(int)Q[4 * p + r].y);
            const u32 c = pkbf(bflo(a) - z0, bfhi(a) - z1);
            W[4 * p + r] = pkbf(bflo(c) * s0, bfhi(c) * s1);
          }
          pin();
        }
        STAMP(2);
        const Sel sel = mksel(lane);
        {
          const u32 off = WS_XR + ((144 * w >> 6) + lane) * 1024;
          bool ok = lane >= 3;
#pragma nounroll
          for (int it = 0; it < SPIN_MEM; ++it) {
            cbar();
            if (!ok) ok = tagok(ldg4(rw, off), tag);
            if (__all(ok)) break;
            __builtin_amdgcn_s_sleep(1);
          }
        }
        STAMP(3);
        v4u g[9];
        const u32 goff = WS_XR + ((36 * w + q) * 4 + b) * 16;
#pragma unroll
        for (int p = 0; p < 9; ++p) g[p] = ldg4(rw, goff + p * 256);
        fix_granules<9>(g, rw, goff, 256, tag);
        STAMP(4);
        v4f acc = {0, 0, 0, 0};
#pragma unroll
        for (int p = 0; p < 9; ++p) {
          const v4u a = {perm(g[p].x, sel.aLo), perm(g[p].x, sel.aHi), perm(g[p].z, sel.aLo), perm(g[p].z, sel.aHi)};
          acc = mfma32(a, v4u{W[4 * p], W[4 * p + 1], W[4 * p + 2], W[4 * p + 3]}, acc);
        }
        if (lane < 32) {
          float* dst = &L.redC[w][2 * n + q];
#pragma unroll
          for (int r = 0; r < 4; ++r) dst[r * 32] = acc[r];
        }
        if (arrive_last(&L.cnt[0], 4, lane)) {
          if (lane < 32) {
            const int Qd = lane >> 2;
            v4f s = {0, 0, 0, 0};
#pragma unroll
            for (int k = 0; k < 4; ++k) s += *(const v4f*)&L.redC[k][b * 32 + 4 * Qd];
            stg4(rw, WS_H + id * 512 + lane * 16, v4u{pkbf(s[0], s[1]), tag, pkbf(s[2], s[3]), tag});
          }
          STAMP(5);
          watch_h(L, rw, tag, 16 * (id & 3), 16, lane);
          STAMP(6);
        }
      }
    }
  } else if (w == 0) {
    __builtin_amdgcn_s_setprio(3);
    u32 E = __builtin_amdgcn_raw_buffer_load_b32(rw, WS_E, 0, SC1);
    waitlgkm();
    wgbar();
    const u32 tag = __builtin_amdgcn_readfirstlane(E) + 1;
    if (lane == 0) __builtin_amdgcn_raw_buffer_store_b64(v2u{tag, tag}, rw, WS_ACK + wg * 8, 0, SC1);
    STAMP(1);
    if (role == 5) {
#pragma nounroll
      for (int it = 0; it < SPIN_MEM; ++it) {
        cbar();
        const v4u a0 = ldg4(rw, WS_ACK + lane * 16), a1 = ldg4(rw, WS_ACK + 1024 + lane * 16);
        const bool ok = a0.x == tag && a0.y == tag && a0.z == tag && a0.w == tag && a1.x == tag && a1.y == tag && a1.z == tag && a1.w == tag;
        if (__all(ok)) break;
        __builtin_amdgcn_s_sleep(8);
      }
      cbar();
      if (lane == 0) __builtin_amdgcn_raw_buffer_store_b32(tag, rw, WS_E, 0, SC1);
      STAMP(2);
    } else {
      watch_h(L, rw, tag, role == 4 ? 88 : role == 2 ? 64 : 80, role == 4 ? 56 : role == 2 ? 24 : 8, lane);
      STAMP(2);
      if (role == 4) {
      #pragma nounroll
  for (int it = 0; it < SPIN_LDS; ++it) {
          if (__builtin_amdgcn_readfirstlane(__hip_atomic_load(&L.nearEnd, __ATOMIC_RELAXED, __HIP_MEMORY_SCOPE_WORKGROUP))) break;
          __builtin_amdgcn_s_sleep(2);
        }
        cbar();
        STAMP(3);
        const u32 po = WS_P + id * 12288 + lane * 16;
        const bool six = id < 18;
        v4u g[12];
#pragma nounroll
        for (int it = 0; it < SPIN_MEM; ++it) {
          cbar();
#pragma unroll
          for (int k = 0; k < 10; ++k) g[k] = ldg4(rw, po + k * 1024);
          if (six) { g[10] = ldg4(rw, po + 10 * 1024); g[11] = ldg4(rw, po + 11 * 1024); }
          else { g[10] = v4u{0, tag, 0, tag}; g[11] = g[10]; }
          bool ok = true;
#pragma unroll
          for (int k = 0; k < 12; ++k) ok = ok && tagok(g[k], tag);
          if (__all(ok)) break;
          __builtin_amdgcn_s_sleep(1);
        }
        STAMP(4);
        v4f s = {0, 0, 0, 0};
#pragma unroll
        for (int m = 0; m < 6; ++m) s += v4f{bitsf(g[2 * m].x), bitsf(g[2 * m].z), bitsf(g[2 * m + 1].x), bitsf(g[2 * m + 1].z)};
        *(v4f*)&L.redV[0][(lane & 3) * 64 + 4 * (lane >> 2)] = s;
        if (arrive_last(&L.cnt[1], 8, lane)) { finish(L, out, id * 64, lane); STAMP(6); }
      }
    }
  } else if (role == 4) {
    v128<KF, PF>(L, rv, rw, out, id * 64, 2816 + (w - 1) * 256, w, lane STPASS);
  } else if (role != 5 && w >= 2) {
    const int vw = w - 2;
    if (role == 2) v256<KA, PN>(L, rv, rw, id, (16 + vw) * 128, w, lane, true, 0, 2, 6, 4 STPASS);
    else v256<KA, PN>(L, rv, rw, 3 * id + (vw >> 1), (20 + (vw & 1)) * 128, w, lane, true, vw >> 1, 2 + (vw & 6), 2, 5 STPASS);
  } else {
    wgbar();
  }
}

}

extern "C" void kernel_launch(void* const* d_in, const int* in_sizes, int n_in, void* d_out, int out_size, void* d_ws,
                              size_t ws_size, hipStream_t stream) {
  qk::quip_kernel<<<256, 512, 0, stream>>>((const unsigned short*)d_in[0], (const int*)d_in[1], (const unsigned short*)d_in[2],
                                          (const unsigned short*)d_in[3], (const unsigned short*)d_in[4],
                                          (const unsigned short*)d_in[5], (unsigned short*)d_out, (unsigned char*)d_ws);
}
```

```cpp
#include <hip/hip_runtime.h>
#include <hip/hip_bf16.h>

namespace qk {

typedef unsigned u32;
typedef unsigned long long u64;
typedef u32 v4u __attribute__((ext_vector_type(4)));
typedef u32 v2u __attribute__((ext_vector_type(2)));
typedef float v4f __attribute__((ext_vector_type(4)));
typedef float v2f __attribute__((ext_vector_type(2)));
typedef short v4s __attribute__((ext_vector_type(4)));
typedef __bf16 v8bf __attribute__((ext_vector_type(8)));
typedef __bf16 v2bf __attribute__((ext_vector_type(2)));
typedef __amdgpu_buffer_rsrc_t rsrc_t;

constexpr int D = 576, F = 4608;
constexpr int VP = F * 2;
constexpr int QP = F * 4;
constexpr int UP = D * 2;

#define KN 5
#define KA 4
#define KF 3
#define PN 32
#define PF 31
#define NEARF 31
#define AUXV 2
#define AUXQ 2
constexpr int SC1 = 16;

constexpr u32 WS_E = 0;
constexpr u32 WS_ACK = 4096;
constexpr u32 WS_XR = 8192;
constexpr u32 WS_H = 32768;
constexpr u32 WS_P = 131072;
constexpr u32 WS_BYTES = WS_P + 72 * 12288;
constexpr u32 WS_DBG = 2u << 20;

constexpr int SPIN_LDS = 1 << 22;
constexpr int SPIN_MEM = 1 << 18;

struct __attribute__((aligned(16))) Lds {
  u64 hflag;
  u32 nearEnd;
  u32 pad0;
  u32 cnt[4];
  float redC[4][256];
  float redV[8][512];
  v2u himg[8][256];
};

#define STAMP(i)
#define STDECL
#define STARG
#define STPASS

template <int N> __device__ __forceinline__ void waitvm() { asm volatile("s_waitcnt vmcnt(%0)" :: "n"(N) : "memory"); }
__device__ __forceinline__ void waitlgkm() { asm volatile("s_waitcnt lgkmcnt(0)" ::: "memory"); }
__device__ __forceinline__ void cbar() { asm volatile("" ::: "memory"); }
__device__ __forceinline__ void pin() { __builtin_amdgcn_sched_barrier(0); }
__device__ __forceinline__ void wgbar() { cbar(); __builtin_amdgcn_s_barrier(); cbar(); }
__device__ __forceinline__ rsrc_t mkrs(const void* p, u32 bytes) { return __builtin_amdgcn_make_buffer_rsrc((void*)p, 0, (int)bytes, 0x00020000); }
__device__ __forceinline__ u32 fresh(u32 o) { asm volatile("" : "+v"(o) :: "memory"); return o; }
__device__ __forceinline__ v4u ldg4(rsrc_t rs, u32 off) { return __builtin_amdgcn_raw_buffer_load_b128(rs, off, 0, SC1); }
__device__ __forceinline__ void stg4(rsrc_t rs, u32 off, v4u v) { __builtin_amdgcn_raw_buffer_store_b128(v, rs, off, 0, SC1); }
__device__ __forceinline__ u32 pkbf(float a, float b) {
  v2f t = {a, b};
  return __builtin_bit_cast(u32, __builtin_convertvector(t, v2bf));
}
__device__ __forceinline__ float bflo(u32 w) { return __builtin_bit_cast(float, w << 16); }
__device__ __forceinline__ float bfhi(u32 w) { return __builtin_bit_cast(float, w & 0xffff0000u); }
__device__ __forceinline__ u32 perm(u32 w, u32 sel) { return __builtin_amdgcn_perm(w, w, sel); }
__device__ __forceinline__ bool tagok(v4u g, u32 tag) { return g.y == tag && g.w == tag; }
__device__ __forceinline__ u32 fbits(float f) { return __builtin_bit_cast(u32, f); }
__device__ __forceinline__ float bitsf(u32 u) { return __builtin_bit_cast(float, u); }

struct Sel { u32 aLo, bLo, aHi, bHi; };
__device__ __forceinline__ Sel mksel(int lane) {
  const int s = (lane & 15) >> 2;
  Sel r;
  r.aLo = s == 0 ? 0x0c0c0100u : s == 1 ? 0x01000c0cu : 0x0c0c0c0cu;
  r.bLo = s == 2 ? 0x0c0c0100u : s == 3 ? 0x01000c0cu : 0x0c0c0c0cu;
  r.aHi = s == 0 ? 0x0c0c0302u : s == 1 ? 0x03020c0cu : 0x0c0c0c0cu;
  r.bHi = s == 2 ? 0x0c0c0302u : s == 3 ? 0x03020c0cu : 0x0c0c0c0cu;
  return r;
}
__device__ __forceinline__ v4f mfma16(v2u a, v2u b, v4f c) {
  return __builtin_amdgcn_mfma_f32_16x16x16bf16_1k(__builtin_bit_cast(v4s, a), __builtin_bit_cast(v4s, b), c, 0, 0, 0);
}
__device__ __forceinline__ v4f mfma32(v4u a, v4u b, v4f c) {
  return __builtin_amdgcn_mfma_f32_16x16x32_bf16(__builtin_bit_cast(v8bf, a), __builtin_bit_cast(v8bf, b), c, 0, 0, 0);
}

__device__ __forceinline__ bool arrive_last(u32* cnt, u32 n, int lane) {
  waitlgkm();
  u32 old = 0;
  if (lane == 0) old = __hip_atomic_fetch_add(cnt, 1u, __ATOMIC_RELAXED, __HIP_MEMORY_SCOPE_WORKGROUP);
  old = __builtin_amdgcn_readfirstlane(old);
  cbar();
  return old == n - 1;
}
__device__ __forceinline__ u32 wait_hflag(Lds& L) {
  u64 f = 0;
#pragma nounroll
  for (int it = 0; it < SPIN_LDS; ++it) {
    f = __hip_atomic_load(&L.hflag, __ATOMIC_RELAXED, __HIP_MEMORY_SCOPE_WORKGROUP);
    if (__builtin_amdgcn_readfirstlane((u32)(f >> 32))) break;
    __builtin_amdgcn_s_sleep(1);
  }
  cbar();
  return __builtin_amdgcn_readfirstlane((u32)f);
}
__device__ __forceinline__ void watch_h(Lds& L, rsrc_t rw, u32 tag, int strip0, int nstrip, int lane) {
  const u32 off = WS_H + (strip0 + lane) * 512;
  bool ok = lane != 0;
#pragma nounroll
  for (int it = 0; it < SPIN_MEM; ++it) {
    cbar();
    if (!ok) ok = tagok(ldg4(rw, fresh(off)), tag);
    if (__all(ok)) break;
    __builtin_amdgcn_s_sleep(4);
  }
  ok = lane == 0 || lane >= nstrip;
#pragma nounroll
  for (int it = 0; it < SPIN_MEM; ++it) {
    cbar();
    if (!ok) ok = tagok(ldg4(rw, fresh(off)), tag);
    if (__all(ok)) break;
    __builtin_amdgcn_s_sleep(1);
  }
  cbar();
  if (lane == 0) __hip_atomic_store(&L.hflag, (u64)tag | (1ull << 32), __ATOMIC_RELAXED, __HIP_MEMORY_SCOPE_WORKGROUP);
}

template <int N>
__device__ __forceinline__ void fix_granules(v4u (&g)[N], rsrc_t rw, u32 off, u32 stride, u32 tag) {
  bool ok = true;
#pragma unroll
  for (int j = 0; j < N; ++j) ok = ok && tagok(g[j], tag);
  if (__builtin_expect(!__all(ok), 0)) {
#pragma nounroll
    for (int it = 0; it < SPIN_MEM; ++it) {
      cbar();
      __builtin_amdgcn_s_sleep(1);
      ok = true;
      const u32 o = fresh(off);
#pragma unroll
      for (int j = 0; j < N; ++j) { g[j] = ldg4(rw, o + j * stride); }
#pragma unroll
      for (int j = 0; j < N; ++j) ok = ok && tagok(g[j], tag);
      if (__all(ok)) break;
    }
  }
}

template <int K, int P>
__device__ __forceinline__ void v256(Lds& L, rsrc_t rv, rsrc_t rw, int team, int row0, int w, int lane, bool early,
                                     int grp, int gw0, int gn, int member STARG) {
  constexpr int NL = 32;
  const int q = lane >> 4, n = lane & 15, b = lane & 3;
  const u32 voff = (row0 + 4 * q) * VP + team * 256 + n * 16;
  v4u v[NL];
  auto issue = [&](int i) { v[i] = __builtin_amdgcn_raw_buffer_load_b128(rv, voff, (16 * (i >> 2) + (i & 3)) * VP, AUXV); };
  if (!early) wgbar();
#pragma unroll
  for (int i = 0; i < K; ++i) { issue(i); pin(); }
  if (early) wgbar();
  const Sel sel = mksel(lane);
#pragma unroll
  for (int i = K; i < P; ++i) { waitvm<K - 1>(); issue(i); pin(); }
  STAMP(1);
  const u32 tag = wait_hflag(L);
  STAMP(2);
  v4u g[2];
  const u32 hoff = WS_H + row0 * 16 + lane * 16;
  g[0] = ldg4(rw, hoff); g[1] = ldg4(rw, hoff + 1024);
  pin();
  constexpr int P2 = P + K < NL ? P + K : NL;
#pragma unroll
  for (int i = P; i < P2; ++i) { waitvm<K - 1>(); issue(i); pin(); }
  fix_granules<2>(g, rw, hoff, 1024, tag);
  STAMP(3);
  L.himg[w][lane] = v2u{g[0].x, g[0].z};
  L.himg[w][64 + lane] = v2u{g[1].x, g[1].z};
  waitlgkm();
  v2u hq[8];
#pragma unroll
  for (int p = 0; p < 8; ++p) hq[p] = L.himg[w][(4 * p + q) * 4 + b];
  v4f acc0 = {0, 0, 0, 0}, acc1 = {0, 0, 0, 0};
  auto work = [&](int j) {
    const u32 hw = (j & 2) ? hq[j >> 2].y : hq[j >> 2].x;
    const v2u a = (j & 1) ? v2u{perm(hw, sel.aHi), perm(hw, sel.bHi)} : v2u{perm(hw, sel.aLo), perm(hw, sel.bLo)};
    acc0 = mfma16(a, v2u{v[j].x, v[j].y}, acc0);
    acc1 = mfma16(a, v2u{v[j].z, v[j].w}, acc1);
  };
  constexpr int R = NL - P2;
  constexpr int C = R > 0 ? (NL - K + R - 1) / R : 0;
  int done = 0;
#pragma unroll
  for (int i = P2; i < NL; ++i) {
    waitvm<K - 1>(); issue(i); pin();
    const int lim = (i - P2 + 1) * C < i - K + 1 ? (i - P2 + 1) * C : i - K + 1;
#pragma unroll
    for (int j = 0; j < NL; ++j) if (j >= done && j < lim) work(j);
    done = lim;
    pin();
  }
  STAMP(4);
#pragma unroll
  for (int j = 0; j < NL; ++j) if (j >= done) work(j);
  STAMP(5);
  {
    float* dst = &L.redV[w][8 * n + q];
#pragma unroll
    for (int r = 0; r < 4; ++r) { dst[r * 128] = acc0[r]; dst[r * 128 + 4] = acc1[r]; }
  }
  if (arrive_last(&L.cnt[1 + grp], gn, lane)) {
    const int Q = lane >> 2;
    v4f s0 = {0, 0, 0, 0}, s1 = {0, 0, 0, 0};
    for (int k = 0; k < gn; ++k) {
      const float* src = &L.redV[gw0 + k][b * 128 + 4 * Q];
      s0 += *(const v4f*)src;
      s1 += *(const v4f*)(src + 64);
    }
    const u32 po = WS_P + (team * 2) * 12288 + member * 2048 + lane * 16;
    stg4(rw, po, v4u{fbits(s0[0]), tag, fbits(s0[1]), tag});
    stg4(rw, po + 1024, v4u{fbits(s0[2]), tag, fbits(s0[3]), tag});
    stg4(rw, po + 12288, v4u{fbits(s1[0]), tag, fbits(s1[1]), tag});
    stg4(rw, po + 12288 + 1024, v4u{fbits(s1[2]), tag, fbits(s1[3]), tag});
    STAMP(6);
  }
}

__device__ __forceinline__ void finish(Lds& L, unsigned short* __restrict__ out, int col0, int lane) {
  const int Q = lane >> 2, b = lane & 3;
  v4f s = {0, 0, 0, 0};
#pragma unroll
  for (int k = 0; k < 8; ++k) s += *(const v4f*)&L.redV[k][b * 64 + 4 * Q];
  *(v2u*)(out + b * F + col0 + 4 * Q) = v2u{pkbf(s[0], s[1]), pkbf(s[2], s[3])};
}

template <int K, int P>
__device__ __forceinline__ void v128(Lds& L, rsrc_t rv, rsrc_t rw, unsigned short* __restrict__ out, int col0, int row0, int w, int lane STARG) {
  constexpr int NL = 32;
  const int q = lane >> 4, n = lane & 15, b = lane & 3;
  const u32 voff = (row0 + 4 * q) * VP + col0 * 2 + n * 8;
  v2u v[2 * NL];
  auto issue = [&](int i) {
    const int r = 16 * (i >> 1) + 2 * (i & 1);
    v[2 * i] = __builtin_amdgcn_raw_buffer_load_b64(rv, voff, r * VP, AUXV);
    v[2 * i + 1] = __builtin_amdgcn_raw_buffer_load_b64(rv, voff, (r + 1) * VP, AUXV);
  };
#pragma unroll
  for (int i = 0; i < K; ++i) { issue(i); pin(); }
  wgbar();
  const Sel sel = mksel(lane);
#pragma unroll
  for (int i = K; i < P; ++i) { waitvm<2 * K - 2>(); issue(i); pin(); }
  STAMP(1);
  const u32 tag = wait_hflag(L);
  STAMP(2);
  v4u g[4];
  const u32 hoff = WS_H + row0 * 16 + lane * 16;
#pragma unroll
  for (int j = 0; j < 4; ++j) g[j] = ldg4(rw, hoff + j * 1024);
  pin();
  constexpr int P2 = P + K < NL ? P + K : NL;
  auto near = [&](int i) { if (i == NEARF && lane == 0) __hip_atomic_store(&L.nearEnd, 1u, __ATOMIC_RELAXED, __HIP_MEMORY_SCOPE_WORKGROUP); };
  static_assert(NEARF >= P && NEARF < NL, "nearf");
#pragma unroll
  for (int i = P; i < P2; ++i) { waitvm<2 * K - 2>(); issue(i); near(i); pin(); }
  fix_granules<4>(g, rw, hoff, 1024, tag);
  STAMP(3);
#pragma unroll
  for (int j = 0; j < 4; ++j) L.himg[w][64 * j + lane] = v2u{g[j].x, g[j].z};
  waitlgkm();
  v2u hq[16];
#pragma unroll
  for (int p = 0; p < 16; ++p) hq[p] = L.himg[w][(4 * p + q) * 4 + b];
  v4f acc = {0, 0, 0, 0};
  auto work = [&](int j) {
    const u32 hw = (j & 1) ? hq[j >> 1].y : hq[j >> 1].x;
    const v4u a = {perm(hw, sel.aLo), perm(hw, sel.bLo), perm(hw, sel.aHi), perm(hw, sel.bHi)};
    acc = mfma32(a, v4u{v[2 * j].x, v[2 * j].y, v[2 * j + 1].x, v[2 * j + 1].y}, acc);
  };
  constexpr int R = NL - P2;
  constexpr int C = R > 0 ? (NL - K + R - 1) / R : 0;
  int done = 0;
#pragma unroll
  for (int i = P2; i < NL; ++i) {
    waitvm<2 * K - 2>(); issue(i); near(i); pin();
    const int lim = (i - P2 + 1) * C < i - K + 1 ? (i - P2 + 1) * C : i - K + 1;
#pragma unroll
    for (int j = 0; j < NL; ++j) if (j >= done && j < lim) work(j);
    done = lim;
    pin();
  }
  STAMP(4);
#pragma unroll
  for (int j = 0; j < NL; ++j) if (j >= done) work(j);
  STAMP(5);
  {
    float* dst = &L.redV[w][4 * n + q];
#pragma unroll
    for (int r = 0; r < 4; ++r) dst[r * 64] = acc[r];
  }
  if (arrive_last(&L.cnt[1], 8, lane)) { finish(L, out, col0, lane); STAMP(6); }
}

__global__ void __launch_bounds__(512, 2)
quip_kernel(const unsigned short* __restrict__ x, const int* __restrict__ qw, const unsigned short* __restrict__ sc,
            const unsigned short* __restrict__ zr, const unsigned short* __restrict__ U, const unsigned short* __restrict__ V,
            unsigned short* __restrict__ out, unsigned char* ws) {
  __shared__ Lds L;
  STDECL;
  STAMP(0);
  const int tid = threadIdx.x, lane = tid & 63;
  const int w = __builtin_amdgcn_readfirstlane(tid >> 6);
  const int wg = blockIdx.x, xc = wg & 7, j = wg >> 3;
  const rsrc_t rw = mkrs(ws, WS_BYTES);
  const rsrc_t rv = mkrs(V, F * VP);

  int role, id;
  if (j >= 23) { role = 4; id = (j - 23) * 8 + xc; }
  else if (j >= 5) { role = 1; id = (j - 5) * 8 + xc; }
  else if (j < 3 && xc < 3) { role = 0; id = j * 3 + xc; }
  else {
    const int r = j < 3 ? j * 5 + (xc - 3) : 15 + (j - 3) * 8 + xc;
    if (r < 27) { role = 2; id = 9 + r; } else if (r < 30) { role = 3; id = r - 27; } else { role = 5; id = 0; }
  }

  if (w == 0 && lane < 8) ((u32*)&L)[lane] = 0;

  if (role <= 1) {
    if (w >= 4) {
      const int team = role ? id >> 2 : id;
      const int slot = role ? 4 * (id & 3) + (w - 4) : 16 + (w - 4);
      v256<KN, PN>(L, rv, rw, team, slot * 128, w, lane, false, 0, 4, 4, role ? id & 3 : 4 STPASS);
    } else {
      __builtin_amdgcn_s_setprio(3);
      const int q = lane >> 4, n = lane & 15, b = lane & 3;
      u32 E = __builtin_amdgcn_raw_buffer_load_b32(rw, WS_E, 0, SC1);
      pin();
      if (role == 0) {
        const rsrc_t rx = mkrs(x, 4 * D * 2), ru = mkrs(U, D * UP);
        v2u X[9], Ut[36];
        const u32 xoff = b * UP + (144 * w + 4 * q) * 2;
        const u32 uoff = (144 * w + 4 * q) * UP + id * 128 + n * 8;
#pragma unroll
        for (int p = 0; p < 9; ++p) {
          X[p] = __builtin_amdgcn_raw_buffer_load_b64(rx, xoff, p * 32, 0);
#pragma unroll
          for (int r = 0; r < 4; ++r) Ut[4 * p + r] = __builtin_amdgcn_raw_buffer_load_b64(ru, uoff, (16 * p + r) * UP, 0);
          pin();
        }
        const u32 tag = __builtin_amdgcn_readfirstlane(E) + 1;
        waitlgkm();
        wgbar();
        STAMP(1);
        if (w == 0 && lane == 0) __builtin_amdgcn_raw_buffer_store_b64(v2u{tag, tag}, rw, WS_ACK + wg * 8, 0, SC1);
        const Sel sel = mksel(lane);
        v4f acc = {0, 0, 0, 0};
#pragma unroll
        for (int c = 0; c < 18; ++c) {
          const u32 hw = (c & 1) ? X[c >> 1].y : X[c >> 1].x;
          const v4u a = {perm(hw, sel.aLo), perm(hw, sel.bLo), perm(hw, sel.aHi), perm(hw, sel.bHi)};
          acc = mfma32(a, v4u{Ut[2 * c].x, Ut[2 * c].y, Ut[2 * c + 1].x, Ut[2 * c + 1].y}, acc);
        }
        STAMP(2);
        float* dst = &L.redC[w][4 * n + q];
#pragma unroll
        for (int r = 0; r < 4; ++r) dst[r * 64] = acc[r];
        if (arrive_last(&L.cnt[0], 4, lane)) {
          const int Q = lane >> 2;
          v4f s = {0, 0, 0, 0};
#pragma unroll
          for (int k = 0; k < 4; ++k) s += *(const v4f*)&L.redC[k][b * 64 + 4 * Q];
          stg4(rw, WS_XR + id * 1024 + lane * 16, v4u{pkbf(s[0], s[1]), tag, pkbf(s[2], s[3]), tag});
          STAMP(3);
          watch_h(L, rw, tag, 64, 16, lane);
          STAMP(4);
        }
      } else {
        const rsrc_t rq = mkrs(qw, D * QP), rsn = mkrs(sc, 18 * VP), rz = mkrs(zr, 18 * VP);
        u32 Z[9], S[9];
        v2u Q[36];
        const u32 soff = id * 64 + n * 4;
        const u32 qoff = (144 * w + 4 * q) * QP + id * 128 + n * 8;
#pragma unroll
        for (int p = 0; p < 9; ++p) {
          const int grp = (144 * w + 16 * p) >> 5;
          Z[p] = __builtin_amdgcn_raw_buffer_load_b32(rz, soff, grp * VP, 0);
          S[p] = __builtin_amdgcn_raw_buffer_load_b32(rsn, soff, grp * VP, 0);
#pragma unroll
          for (int r = 0; r < 4; ++r) Q[4 * p + r] = __builtin_amdgcn_raw_buffer_load_b64(rq, qoff, (16 * p + r) * QP, AUXQ);
          pin();
        }
        const u32 tag = __builtin_amdgcn_readfirstlane(E) + 1;
        waitlgkm();
        wgbar();
        STAMP(1);
        if (w == 0 && lane == 0) __builtin_amdgcn_raw_buffer_store_b64(v2u{tag, tag}, rw, WS_ACK + wg * 8, 0, SC1);
        u32 W[36];
#pragma unroll
        for (int p = 0; p < 9; ++p) {
          const float z0 = bflo(Z[p]), z1 = bfhi(Z[p]), s0 = bflo(S[p]), s1 = bfhi(S[p]);
#pragma unroll
          for (int r = 0; r < 4; ++r) {
            const u32 a = pkbf((float)(int)Q[4 * p + r].x, (float)(int)Q[4 * p + r].y);
            const u32 c = pkbf(bflo(a) - z0, bfhi(a) - z1);
            W[4 * p + r] = pkbf(bflo(c) * s0, bfhi(c) * s1);
          }
          pin();
        }
        STAMP(2);
        const Sel sel = mksel(lane);
        {
          const u32 off = WS_XR + ((144 * w >> 6) + lane) * 1024;
          bool ok = lane >= 3;
#pragma nounroll
          for (int it = 0; it < SPIN_MEM; ++it) {
            cbar();
            if (!ok) ok = tagok(ldg4(rw, fresh(off)), tag);
            if (__all(ok)) break;
            __builtin_amdgcn_s_sleep(1);
          }
        }
        STAMP(3);
        v4u g[9];
        const u32 goff = WS_XR + ((36 * w + q) * 4 + b) * 16;
#pragma unroll
        for (int p = 0; p < 9; ++p) g[p] = ldg4(rw, goff + p * 256);
        fix_granules<9>(g, rw, goff, 256, tag);
        STAMP(4);
        v4f acc = {0, 0, 0, 0};
#pragma unroll
        for (int p = 0; p < 9; ++p) {
          const v4u a = {perm(g[p].x, sel.aLo), perm(g[p].x, sel.aHi), perm(g[p].z, sel.aLo), perm(g[p].z, sel.aHi)};
          acc = mfma32(a, v4u{W[4 * p], W[4 * p + 1], W[4 * p + 2], W[4 * p + 3]}, acc);
        }
        if (lane < 32) {
          float* dst = &L.redC[w][2 * n + q];
#pragma unroll
          for (int r = 0; r < 4; ++r) dst[r * 32] = acc[r];
        }
        if (arrive_last(&L.cnt[0], 4, lane)) {
          if (lane < 32) {
            const int Qd = lane >> 2;
            v4f s = {0, 0, 0, 0};
#pragma unroll
            for (int k = 0; k < 4; ++k) s += *(const v4f*)&L.redC[k][b * 32 + 4 * Qd];
            stg4(rw, WS_H + id * 512 + lane * 16, v4u{pkbf(s[0], s[1]), tag, pkbf(s[2], s[3]), tag});
          }
          STAMP(5);
          watch_h(L, rw, tag, 16 * (id & 3), 16, lane);
          STAMP(6);
        }
      }
    }
  } else if (w == 0) {
    __builtin_amdgcn_s_setprio(3);
    u32 E = __builtin_amdgcn_raw_buffer_load_b32(rw, WS_E, 0, SC1);
    waitlgkm();
    wgbar();
    const u32 tag = __builtin_amdgcn_readfirstlane(E) + 1;
    if (lane == 0) __builtin_amdgcn_raw_buffer_store_b64(v2u{tag, tag}, rw, WS_ACK + wg * 8, 0, SC1);
    STAMP(1);
    if (role == 5) {
#pragma nounroll
      for (int it = 0; it < SPIN_MEM; ++it) {
        cbar();
        const u32 o = fresh(WS_ACK + lane * 16);
        const v4u a0 = ldg4(rw, o), a1 = ldg4(rw, o + 1024);
        const bool ok = a0.x == tag && a0.y == tag && a0.z == tag && a0.w == tag && a1.x == tag && a1.y == tag && a1.z == tag && a1.w == tag;
        if (__all(ok)) break;
        __builtin_amdgcn_s_sleep(8);
      }
      cbar();
      if (lane == 0) __builtin_amdgcn_raw_buffer_store_b32(tag, rw, WS_E, 0, SC1);
      STAMP(2);
    } else {
      watch_h(L, rw, tag, role == 4 ? 88 : role == 2 ? 64 : 80, role == 4 ? 56 : role == 2 ? 24 : 8, lane);
      STAMP(2);
      if (role == 4) {
      #pragma nounroll
  for (int it = 0; it < SPIN_LDS; ++it) {
          if (__builtin_amdgcn_readfirstlane(__hip_atomic_load(&L.nearEnd, __ATOMIC_RELAXED, __HIP_MEMORY_SCOPE_WORKGROUP))) break;
          __builtin_amdgcn_s_sleep(2);
        }
        cbar();
        STAMP(3);
        const u32 po = WS_P + id * 12288 + lane * 16;
        const bool six = id < 18;
        v4u g[12];
#pragma nounroll
        for (int it = 0; it < SPIN_MEM; ++it) {
          const u32 o = fresh(po);
#pragma unroll
          for (int k = 0; k < 10; ++k) g[k] = ldg4(rw, o + k * 1024);
          if (six) { g[10] = ldg4(rw, o + 10 * 1024); g[11] = ldg4(rw, o + 11 * 1024); }
          else { g[10] = v4u{0, tag, 0, tag}; g[11] = g[10]; }
          bool ok = true;
#pragma unroll
          for (int k = 0; k < 12; ++k) ok = ok && tagok(g[k], tag);
          if (__all(ok)) break;
          __builtin_amdgcn_s_sleep(1);
        }
        STAMP(4);
        v4f s = {0, 0, 0, 0};
#pragma unroll
        for (int m = 0; m < 6; ++m) s += v4f{bitsf(g[2 * m].x), bitsf(g[2 * m].z), bitsf(g[2 * m + 1].x), bitsf(g[2 * m + 1].z)};
        *(v4f*)&L.redV[0][(lane & 3) * 64 + 4 * (lane >> 2)] = s;
        if (arrive_last(&L.cnt[1], 8, lane)) { finish(L, out, id * 64, lane); STAMP(6); }
      }
    }
  } else if (role == 4) {
    v128<KF, PF>(L, rv, rw, out, id * 64, 2816 + (w - 1) * 256, w, lane STPASS);
  } else if (role != 5 && w >= 2) {
    const int vw = w - 2;
    if (role == 2) v256<KA, PN>(L, rv, rw, id, (16 + vw) * 128, w, lane, true, 0, 2, 6, 4 STPASS);
    else v256<KA, PN>(L, rv, rw, 3 * id + (vw >> 1), (20 + (vw & 1)) * 128, w, lane, true, vw >> 1, 2 + (vw & 6), 2, 5 STPASS);
  } else {
    wgbar();
  }
}

}

extern "C" void kernel_launch(void* const* d_in, const int* in_sizes, int n_in, void* d_out, int out_size, void* d_ws,
                              size_t ws_size, hipStream_t stream) {
  qk::quip_kernel<<<256, 512, 0, stream>>>((const unsigned short*)d_in[0], (const int*)d_in[1], (const unsigned short*)d_in[2],
                                          (const unsigned short*)d_in[3], (const unsigned short*)d_in[4],
                                          (const unsigned short*)d_in[5], (unsigned short*)d_out, (unsigned char*)d_ws);
}
```

```cpp
#include <hip/hip_runtime.h>
#include <hip/hip_bf16.h>

namespace qk {

typedef unsigned u32;
typedef unsigned long long u64;
typedef u32 v4u __attribute__((ext_vector_type(4)));
typedef u32 v2u __attribute__((ext_vector_type(2)));
typedef float v4f __attribute__((ext_vector_type(4)));
typedef float v2f __attribute__((ext_vector_type(2)));
typedef short v4s __attribute__((ext_vector_type(4)));
typedef __bf16 v8bf __attribute__((ext_vector_type(8)));
typedef __bf16 v2bf __attribute__((ext_vector_type(2)));
typedef __amdgpu_buffer_rsrc_t rsrc_t;

constexpr int D = 576, F = 4608;
constexpr int VP = F * 2;
constexpr int QP = F * 4;
constexpr int UP = D * 2;

#define KN 5
#define KA 4
#define KF 3
#define PN 32
#define PF 31
#define NEARF 31
#define AUXV 0
#define AUXQ 0
constexpr int SC1 = 16;

constexpr u32 WS_E = 0;
constexpr u32 WS_ACK = 4096;
constexpr u32 WS_XR = 8192;
constexpr u32 WS_H = 32768;
constexpr u32 WS_P = 131072;
constexpr u32 WS_BYTES = WS_P + 72 * 12288;
constexpr u32 WS_DBG = 2u << 20;

constexpr int SPIN_LDS = 1 << 22;
constexpr int SPIN_MEM = 1 << 18;

struct __attribute__((aligned(16))) Lds {
  u64 hflag;
  u32 nearEnd;
  u32 pad0;
  u32 cnt[4];
  float redC[4][256];
  float redV[8][512];
  v2u himg[8][256];
};

#define STAMP(i)
#define STDECL
#define STARG
#define STPASS

template <int N> __device__ __forceinline__ void waitvm() { asm volatile("s_waitcnt vmcnt(%0)" :: "n"(N) : "memory"); }
__device__ __forceinline__ void waitlgkm() { asm volatile("s_waitcnt lgkmcnt(0)" ::: "memory"); }
__device__ __forceinline__ void cbar() { asm volatile("" ::: "memory"); }
__device__ __forceinline__ void pin() { __builtin_amdgcn_sched_barrier(0); }
__device__ __forceinline__ void wgbar() { cbar(); __builtin_amdgcn_s_barrier(); cbar(); }
__device__ __forceinline__ rsrc_t mkrs(const void* p, u32 bytes) { return __builtin_amdgcn_make_buffer_rsrc((void*)p, 0, (int)bytes, 0x00020000); }
__device__ __forceinline__ u32 fresh(u32 o) { asm volatile("" : "+v"(o) :: "memory"); return o; }
__device__ __forceinline__ v4u ldg4(rsrc_t rs, u32 off) { return __builtin_amdgcn_raw_buffer_load_b128(rs, off, 0, SC1); }
__device__ __forceinline__ void stg4(rsrc_t rs, u32 off, v4u v) { __builtin_amdgcn_raw_buffer_store_b128(v, rs, off, 0, SC1); }
__device__ __forceinline__ u32 pkbf(float a, float b) {
  v2f t = {a, b};
  return __builtin_bit_cast(u32, __builtin_convertvector(t, v2bf));
}
__device__ __forceinline__ float bflo(u32 w) { return __builtin_bit_cast(float, w << 16); }
__device__ __forceinline__ float bfhi(u32 w) { return __builtin_bit_cast(float, w & 0xffff0000u); }
__device__ __forceinline__ u32 perm(u32 w, u32 sel) { return __builtin_amdgcn_perm(w, w, sel); }
__device__ __forceinline__ bool tagok(v4u g, u32 tag) { return g.y == tag && g.w == tag; }
__device__ __forceinline__ u32 fbits(float f) { return __builtin_bit_cast(u32, f); }
__device__ __forceinline__ float bitsf(u32 u) { return __builtin_bit_cast(float, u); }

struct Sel { u32 aLo, bLo, aHi, bHi; };
__device__ __forceinline__ Sel mksel(int lane) {
  const int s = (lane & 15) >> 2;
  Sel r;
  r.aLo = s == 0 ? 0x0c0c0100u : s == 1 ? 0x01000c0cu : 0x0c0c0c0cu;
  r.bLo = s == 2 ? 0x0c0c0100u : s == 3 ? 0x01000c0cu : 0x0c0c0c0cu;
  r.aHi = s == 0 ? 0x0c0c0302u : s == 1 ? 0x03020c0cu : 0x0c0c0c0cu;
  r.bHi = s == 2 ? 0x0c0c0302u : s == 3 ? 0x03020c0cu : 0x0c0c0c0cu;
  return r;
}
__device__ __forceinline__ v4f mfma16(v2u a, v2u b, v4f c) {
  return __builtin_amdgcn_mfma_f32_16x16x16bf16_1k(__builtin_bit_cast(v4s, a), __builtin_bit_cast(v4s, b), c, 0, 0, 0);
}
__device__ __forceinline__ v4f mfma32(v4u a, v4u b, v4f c) {
  return __builtin_amdgcn_mfma_f32_16x16x32_bf16(__builtin_bit_cast(v8bf, a), __builtin_bit_cast(v8bf, b), c, 0, 0, 0);
}

__device__ __forceinline__ bool arrive_last(u32* cnt, u32 n, int lane) {
  waitlgkm();
  u32 old = 0;
  if (lane == 0) old = __hip_atomic_fetch_add(cnt, 1u, __ATOMIC_RELAXED, __HIP_MEMORY_SCOPE_WORKGROUP);
  old = __builtin_amdgcn_readfirstlane(old);
  cbar();
  return old == n - 1;
}
__device__ __forceinline__ u32 wait_hflag(Lds& L) {
  u64 f = 0;
#pragma nounroll
  for (int it = 0; it < SPIN_LDS; ++it) {
    f = __hip_atomic_load(&L.hflag, __ATOMIC_RELAXED, __HIP_MEMORY_SCOPE_WORKGROUP);
    if (__builtin_amdgcn_readfirstlane((u32)(f >> 32))) break;
    __builtin_amdgcn_s_sleep(1);
  }
  cbar();
  return __builtin_amdgcn_readfirstlane((u32)f);
}
__device__ __forceinline__ void watch_h(Lds& L, rsrc_t rw, u32 tag, int strip0, int nstrip, int lane) {
  const u32 off = WS_H + (strip0 + lane) * 512;
  bool ok = lane != 0;
#pragma nounroll
  for (int it = 0; it < SPIN_MEM; ++it) {
    cbar();
    if (!ok) ok = tagok(ldg4(rw, fresh(off)), tag);
    if (__all(ok)) break;
    __builtin_amdgcn_s_sleep(4);
  }
  ok = lane == 0 || lane >= nstrip;
#pragma nounroll
  for (int it = 0; it < SPIN_MEM; ++it) {
    cbar();
    if (!ok) ok = tagok(ldg4(rw, fresh(off)), tag);
    if (__all(ok)) break;
    __builtin_amdgcn_s_sleep(1);
  }
  cbar();
  if (lane == 0) __hip_atomic_store(&L.hflag, (u64)tag | (1ull << 32), __ATOMIC_RELAXED, __HIP_MEMORY_SCOPE_WORKGROUP);
}

template <int N>
__device__ __forceinline__ void fix_granules(v4u (&g)[N], rsrc_t rw, u32 off, u32 stride, u32 tag) {
  bool ok = true;
#pragma unroll
  for (int j = 0; j < N; ++j) ok = ok && tagok(g[j], tag);
  if (__builtin_expect(!__all(ok), 0)) {
#pragma nounroll
    for (int it = 0; it < SPIN_MEM; ++it) {
      cbar();
      __builtin_amdgcn_s_sleep(1);
      ok = true;
      const u32 o = fresh(off);
#pragma unroll
      for (int j = 0; j < N; ++j) { g[j] = ldg4(rw, o + j * stride); }
#pragma unroll
      for (int j = 0; j < N; ++j) ok = ok && tagok(g[j], tag);
      if (__all(ok)) break;
    }
  }
}

template <int K, int P>
__device__ __forceinline__ void v256(Lds& L, rsrc_t rv, rsrc_t rw, int team, int row0, int w, int lane, bool early,
                                     int grp, int gw0, int gn, int member STARG) {
  constexpr int NL = 32;
  const int q = lane >> 4, n = lane & 15, b = lane & 3;
  const u32 voff = (row0 + 4 * q) * VP + team * 256 + n * 16;
  v4u v[NL];
  auto issue = [&](int i) { v[i] = __builtin_amdgcn_raw_buffer_load_b128(rv, voff, (16 * (i >> 2) + (i & 3)) * VP, AUXV); };
  if (!early) wgbar();
#pragma unroll
  for (int i = 0; i < K; ++i) { issue(i); pin(); }
  if (early) wgbar();
  const Sel sel = mksel(lane);
#pragma unroll
  for (int i = K; i < P; ++i) { waitvm<K - 1>(); issue(i); pin(); }
  STAMP(1);
  const u32 tag = wait_hflag(L);
  STAMP(2);
  v4u g[2];
  const u32 hoff = WS_H + row0 * 16 + lane * 16;
  g[0] = ldg4(rw, hoff); g[1] = ldg4(rw, hoff + 1024);
  pin();
  constexpr int P2 = P + K < NL ? P + K : NL;
#pragma unroll
  for (int i = P; i < P2; ++i) { waitvm<K - 1>(); issue(i); pin(); }
  fix_granules<2>(g, rw, hoff, 1024, tag);
  STAMP(3);
  L.himg[w][lane] = v2u{g[0].x, g[0].z};
  L.himg[w][64 + lane] = v2u{g[1].x, g[1].z};
  waitlgkm();
  v2u hq[8];
#pragma unroll
  for (int p = 0; p < 8; ++p) hq[p] = L.himg[w][(4 * p + q) * 4 + b];
  v4f acc0 = {0, 0, 0, 0}, acc1 = {0, 0, 0, 0};
  auto work = [&](int j) {
    const u32 hw = (j & 2) ? hq[j >> 2].y : hq[j >> 2].x;
    const v2u a = (j & 1) ? v2u{perm(hw, sel.aHi), perm(hw, sel.bHi)} : v2u{perm(hw, sel.aLo), perm(hw, sel.bLo)};
    acc0 = mfma16(a, v2u{v[j].x, v[j].y}, acc0);
    acc1 = mfma16(a, v2u{v[j].z, v[j].w}, acc1);
  };
  constexpr int R = NL - P2;
  constexpr int C = R > 0 ? (NL - K + R - 1) / R : 0;
  int done = 0;
#pragma unroll
  for (int i = P2; i < NL; ++i) {
    waitvm<K - 1>(); issue(i); pin();
    const int lim = (i - P2 + 1) * C < i - K + 1 ? (i - P2 + 1) * C : i - K + 1;
#pragma unroll
    for (int j = 0; j < NL; ++j) if (j >= done && j < lim) work(j);
    done = lim;
    pin();
  }
  STAMP(4);
#pragma unroll
  for (int j = 0; j < NL; ++j) if (j >= done) work(j);
  STAMP(5);
  {
    float* dst = &L.redV[w][8 * n + q];
#pragma unroll
    for (int r = 0; r < 4; ++r) { dst[r * 128] = acc0[r]; dst[r * 128 + 4] = acc1[r]; }
  }
  if (arrive_last(&L.cnt[1 + grp], gn, lane)) {
    const int Q = lane >> 2;
    v4f s0 = {0, 0, 0, 0}, s1 = {0, 0, 0, 0};
    for (int k = 0; k < gn; ++k) {
      const float* src = &L.redV[gw0 + k][b * 128 + 4 * Q];
      s0 += *(const v4f*)src;
      s1 += *(const v4f*)(src + 64);
    }
    const u32 po = WS_P + (team * 2) * 12288 + member * 2048 + lane * 16;
    stg4(rw, po, v4u{fbits(s0[0]), tag, fbits(s0[1]), tag});
    stg4(rw, po + 1024, v4u{fbits(s0[2]), tag, fbits(s0[3]), tag});
    stg4(rw, po + 12288, v4u{fbits(s1[0]), tag, fbits(s1[1]), tag});
    stg4(rw, po + 12288 + 1024, v4u{fbits(s1[2]), tag, fbits(s1[3]), tag});
    STAMP(6);
  }
}

__device__ __forceinline__ void finish(Lds& L, unsigned short* __restrict__ out, int col0, int lane) {
  const int Q = lane >> 2, b = lane & 3;
  v4f s = {0, 0, 0, 0};
#pragma unroll
  for (int k = 0; k < 8; ++k) s += *(const v4f*)&L.redV[k][b * 64 + 4 * Q];
  *(v2u*)(out + b * F + col0 + 4 * Q) = v2u{pkbf(s[0], s[1]), pkbf(s[2], s[3])};
}

template <int K, int P>
__device__ __forceinline__ void v128(Lds& L, rsrc_t rv, rsrc_t rw, unsigned short* __restrict__ out, int col0, int row0, int w, int lane STARG) {
  constexpr int NL = 32;
  const int q = lane >> 4, n = lane & 15, b = lane & 3;
  const u32 voff = (row0 + 4 * q) * VP + col0 * 2 + n * 8;
  v2u v[2 * NL];
  auto issue = [&](int i) {
    const int r = 16 * (i >> 1) + 2 * (i & 1);
    v[2 * i] = __builtin_amdgcn_raw_buffer_load_b64(rv, voff, r * VP, AUXV);
    v[2 * i + 1] = __builtin_amdgcn_raw_buffer_load_b64(rv, voff, (r + 1) * VP, AUXV);
  };
#pragma unroll
  for (int i = 0; i < K; ++i) { issue(i); pin(); }
  wgbar();
  const Sel sel = mksel(lane);
#pragma unroll
  for (int i = K; i < P; ++i) { waitvm<2 * K - 2>(); issue(i); pin(); }
  STAMP(1);
  const u32 tag = wait_hflag(L);
  STAMP(2);
  v4u g[4];
  const u32 hoff = WS_H + row0 * 16 + lane * 16;
#pragma unroll
  for (int j = 0; j < 4; ++j) g[j] = ldg4(rw, hoff + j * 1024);
  pin();
  constexpr int P2 = P + K < NL ? P + K : NL;
  auto near = [&](int i) { if (i == NEARF && lane == 0) __hip_atomic_store(&L.nearEnd, 1u, __ATOMIC_RELAXED, __HIP_MEMORY_SCOPE_WORKGROUP); };
  static_assert(NEARF >= P && NEARF < NL, "nearf");
#pragma unroll
  for (int i = P; i < P2; ++i) { waitvm<2 * K - 2>(); issue(i); near(i); pin(); }
  fix_granules<4>(g, rw, hoff, 1024, tag);
  STAMP(3);
#pragma unroll
  for (int j = 0; j < 4; ++j) L.himg[w][64 * j + lane] = v2u{g[j].x, g[j].z};
  waitlgkm();
  v2u hq[16];
#pragma unroll
  for (int p = 0; p < 16; ++p) hq[p] = L.himg[w][(4 * p + q) * 4 + b];
  v4f acc = {0, 0, 0, 0};
  auto work = [&](int j) {
    const u32 hw = (j & 1) ? hq[j >> 1].y : hq[j >> 1].x;
    const v4u a = {perm(hw, sel.aLo), perm(hw, sel.bLo), perm(hw, sel.aHi), perm(hw, sel.bHi)};
    acc = mfma32(a, v4u{v[2 * j].x, v[2 * j].y, v[2 * j + 1].x, v[2 * j + 1].y}, acc);
  };
  constexpr int R = NL - P2;
  constexpr int C = R > 0 ? (NL - K + R - 1) / R : 0;
  int done = 0;
#pragma unroll
  for (int i = P2; i < NL; ++i) {
    waitvm<2 * K - 2>(); issue(i); near(i); pin();
    const int lim = (i - P2 + 1) * C < i - K + 1 ? (i - P2 + 1) * C : i - K + 1;
#pragma unroll
    for (int j = 0; j < NL; ++j) if (j >= done && j < lim) work(j);
    done = lim;
    pin();
  }
  STAMP(4);
#pragma unroll
  for (int j = 0; j < NL; ++j) if (j >= done) work(j);
  STAMP(5);
  {
    float* dst = &L.redV[w][4 * n + q];
#pragma unroll
    for (int r = 0; r < 4; ++r) dst[r * 64] = acc[r];
  }
  if (arrive_last(&L.cnt[1], 8, lane)) { finish(L, out, col0, lane); STAMP(6); }
}

__global__ void __launch_bounds__(512, 2)
quip_kernel(const unsigned short* __restrict__ x, const int* __restrict__ qw, const unsigned short* __restrict__ sc,
            const unsigned short* __restrict__ zr, const unsigned short* __restrict__ U, const unsigned short* __restrict__ V,
            unsigned short* __restrict__ out, unsigned char* ws) {
  __shared__ Lds L;
  const u64 tstart = __builtin_amdgcn_s_memrealtime();
  STDECL;
  STAMP(0);
  const int tid = threadIdx.x, lane = tid & 63;
  const int w = __builtin_amdgcn_readfirstlane(tid >> 6);
  const int wg = blockIdx.x, xc = wg & 7, j = wg >> 3;
  const rsrc_t rw = mkrs(ws, WS_BYTES);
  const rsrc_t rv = mkrs(V, F * VP);

  int role, id;
  if (j >= 23) { role = 4; id = (j - 23) * 8 + xc; }
  else if (j >= 5) { role = 1; id = (j - 5) * 8 + xc; }
  else if (j < 3 && xc < 3) { role = 0; id = j * 3 + xc; }
  else {
    const int r = j < 3 ? j * 5 + (xc - 3) : 15 + (j - 3) * 8 + xc;
    if (r < 27) { role = 2; id = 9 + r; } else if (r < 30) { role = 3; id = r - 27; } else { role = 5; id = 0; }
  }

  if (w == 0 && lane < 8) ((u32*)&L)[lane] = 0;

  if (role <= 1) {
    if (w >= 4) {
      const int team = role ? id >> 2 : id;
      const int slot = role ? 4 * (id & 3) + (w - 4) : 16 + (w - 4);
      v256<KN, PN>(L, rv, rw, team, slot * 128, w, lane, false, 0, 4, 4, role ? id & 3 : 4 STPASS);
    } else {
      __builtin_amdgcn_s_setprio(3);
      const int q = lane >> 4, n = lane & 15, b = lane & 3;
      u32 E = __builtin_amdgcn_raw_buffer_load_b32(rw, WS_E, 0, SC1);
      pin();
      if (role == 0) {
        const rsrc_t rx = mkrs(x, 4 * D * 2), ru = mkrs(U, D * UP);
        v2u X[9], Ut[36];
        const u32 xoff = b * UP + (144 * w + 4 * q) * 2;
        const u32 uoff = (144 * w + 4 * q) * UP + id * 128 + n * 8;
#pragma unroll
        for (int p = 0; p < 9; ++p) {
          X[p] = __builtin_amdgcn_raw_buffer_load_b64(rx, xoff, p * 32, 0);
#pragma unroll
          for (int r = 0; r < 4; ++r) Ut[4 * p + r] = __builtin_amdgcn_raw_buffer_load_b64(ru, uoff, (16 * p + r) * UP, 0);
          pin();
        }
        const u32 tag = __builtin_amdgcn_readfirstlane(E) + 1;
        waitlgkm();
        wgbar();
        STAMP(1);
        if (w == 0 && lane == 0) __builtin_amdgcn_raw_buffer_store_b64(v2u{tag, tag}, rw, WS_ACK + wg * 8, 0, SC1);
        const Sel sel = mksel(lane);
        v4f acc = {0, 0, 0, 0};
#pragma unroll
        for (int c = 0; c < 18; ++c) {
          const u32 hw = (c & 1) ? X[c >> 1].y : X[c >> 1].x;
          const v4u a = {perm(hw, sel.aLo), perm(hw, sel.bLo), perm(hw, sel.aHi), perm(hw, sel.bHi)};
          acc = mfma32(a, v4u{Ut[2 * c].x, Ut[2 * c].y, Ut[2 * c + 1].x, Ut[2 * c + 1].y}, acc);
        }
        STAMP(2);
        float* dst = &L.redC[w][4 * n + q];
#pragma unroll
        for (int r = 0; r < 4; ++r) dst[r * 64] = acc[r];
        if (arrive_last(&L.cnt[0], 4, lane)) {
          const int Q = lane >> 2;
          v4f s = {0, 0, 0, 0};
#pragma unroll
          for (int k = 0; k < 4; ++k) s += *(const v4f*)&L.redC[k][b * 64 + 4 * Q];
          stg4(rw, WS_XR + id * 1024 + lane * 16, v4u{pkbf(s[0], s[1]), tag, pkbf(s[2], s[3]), tag});
          STAMP(3);
          watch_h(L, rw, tag, 64, 16, lane);
          STAMP(4);
        }
      } else {
        const rsrc_t rq = mkrs(qw, D * QP), rsn = mkrs(sc, 18 * VP), rz = mkrs(zr, 18 * VP);
        u32 Z[9], S[9];
        v2u Q[36];
        const u32 soff = id * 64 + n * 4;
        const u32 qoff = (144 * w + 4 * q) * QP + id * 128 + n * 8;
#pragma unroll
        for (int p = 0; p < 9; ++p) {
          const int grp = (144 * w + 16 * p) >> 5;
          Z[p] = __builtin_amdgcn_raw_buffer_load_b32(rz, soff, grp * VP, 0);
          S[p] = __builtin_amdgcn_raw_buffer_load_b32(rsn, soff, grp * VP, 0);
#pragma unroll
          for (int r = 0; r < 4; ++r) Q[4 * p + r] = __builtin_amdgcn_raw_buffer_load_b64(rq, qoff, (16 * p + r) * QP, AUXQ);
          pin();
        }
        const u32 tag = __builtin_amdgcn_readfirstlane(E) + 1;
        waitlgkm();
        wgbar();
        STAMP(1);
        if (w == 0 && lane == 0) __builtin_amdgcn_raw_buffer_store_b64(v2u{tag, tag}, rw, WS_ACK + wg * 8, 0, SC1);
        u32 W[36];
#pragma unroll
        for (int p = 0; p < 9; ++p) {
          const float z0 = bflo(Z[p]), z1 = bfhi(Z[p]), s0 = bflo(S[p]), s1 = bfhi(S[p]);
#pragma unroll
          for (int r = 0; r < 4; ++r) {
            const u32 a = pkbf((float)(int)Q[4 * p + r].x, (float)(int)Q[4 * p + r].y);
            const u32 c = pkbf(bflo(a) - z0, bfhi(a) - z1);
            W[4 * p + r] = pkbf(bflo(c) * s0, bfhi(c) * s1);
          }
          pin();
        }
        STAMP(2);
        const Sel sel = mksel(lane);
        {
          const u32 off = WS_XR + ((144 * w >> 6) + lane) * 1024;
          bool ok = lane >= 3;
#pragma nounroll
          for (int it = 0; it < SPIN_MEM; ++it) {
            cbar();
            if (!ok) ok = tagok(ldg4(rw, fresh(off)), tag);
            if (__all(ok)) break;
            __builtin_amdgcn_s_sleep(1);
          }
        }
        STAMP(3);
        v4u g[9];
        const u32 goff = WS_XR + ((36 * w + q) * 4 + b) * 16;
#pragma unroll
        for (int p = 0; p < 9; ++p) g[p] = ldg4(rw, goff + p * 256);
        fix_granules<9>(g, rw, goff, 256, tag);
        STAMP(4);
        v4f acc = {0, 0, 0, 0};
#pragma unroll
        for (int p = 0; p < 9; ++p) {
          const v4u a = {perm(g[p].x, sel.aLo), perm(g[p].x, sel.aHi), perm(g[p].z, sel.aLo), perm(g[p].z, sel.aHi)};
          acc = mfma32(a, v4u{W[4 * p], W[4 * p + 1], W[4 * p + 2], W[4 * p + 3]}, acc);
        }
        if (lane < 32) {
          float* dst = &L.redC[w][2 * n + q];
#pragma unroll
          for (int r = 0; r < 4; ++r) dst[r * 32] = acc[r];
        }
        if (arrive_last(&L.cnt[0], 4, lane)) {
          if (lane < 32) {
            const int Qd = lane >> 2;
            v4f s = {0, 0, 0, 0};
#pragma unroll
            for (int k = 0; k < 4; ++k) s += *(const v4f*)&L.redC[k][b * 32 + 4 * Qd];
            stg4(rw, WS_H + id * 512 + lane * 16, v4u{pkbf(s[0], s[1]), tag, pkbf(s[2], s[3]), tag});
          }
          STAMP(5);
          watch_h(L, rw, tag, 16 * (id & 3), 16, lane);
          STAMP(6);
        }
      }
    }
  } else if (w == 0) {
    __builtin_amdgcn_s_setprio(3);
    u32 E = __builtin_amdgcn_raw_buffer_load_b32(rw, WS_E, 0, SC1);
    waitlgkm();
    wgbar();
    const u32 tag = __builtin_amdgcn_readfirstlane(E) + 1;
    if (lane == 0) __builtin_amdgcn_raw_buffer_store_b64(v2u{tag, tag}, rw, WS_ACK + wg * 8, 0, SC1);
    STAMP(1);
    if (role == 5) {
#pragma nounroll
      for (int it = 0; it < SPIN_MEM; ++it) {
        cbar();
        const u32 o = fresh(WS_ACK + lane * 16);
        const v4u a0 = ldg4(rw, o), a1 = ldg4(rw, o + 1024);
        const bool ok = a0.x == tag && a0.y == tag && a0.z == tag && a0.w == tag && a1.x == tag && a1.y == tag && a1.z == tag && a1.w == tag;
        if (__all(ok)) break;
        __builtin_amdgcn_s_sleep(8);
      }
      cbar();
      if (lane == 0) __builtin_amdgcn_raw_buffer_store_b32(tag, rw, WS_E, 0, SC1);
      for (int it = 0; it < 100000; ++it) { if (__builtin_amdgcn_s_memrealtime() - tstart >= 3000) break; __builtin_amdgcn_s_sleep(1); }
      STAMP(2);
    } else {
      watch_h(L, rw, tag, role == 4 ? 88 : role == 2 ? 64 : 80, role == 4 ? 56 : role == 2 ? 24 : 8, lane);
      STAMP(2);
      if (role == 4) {
      #pragma nounroll
  for (int it = 0; it < SPIN_LDS; ++it) {
          if (__builtin_amdgcn_readfirstlane(__hip_atomic_load(&L.nearEnd, __ATOMIC_RELAXED, __HIP_MEMORY_SCOPE_WORKGROUP))) break;
          __builtin_amdgcn_s_sleep(2);
        }
        cbar();
        STAMP(3);
        const u32 po = WS_P + id * 12288 + lane * 16;
        const bool six = id < 18;
        v4u g[12];
#pragma nounroll
        for (int it = 0; it < SPIN_MEM; ++it) {
          const u32 o = fresh(po);
#pragma unroll
          for (int k = 0; k < 10; ++k) g[k] = ldg4(rw, o + k * 1024);
          if (six) { g[10] = ldg4(rw, o + 10 * 1024); g[11] = ldg4(rw, o + 11 * 1024); }
          else { g[10] = v4u{0, tag, 0, tag}; g[11] = g[10]; }
          bool ok = true;
#pragma unroll
          for (int k = 0; k < 12; ++k) ok = ok && tagok(g[k], tag);
          if (__all(ok)) break;
          __builtin_amdgcn_s_sleep(1);
        }
        STAMP(4);
        v4f s = {0, 0, 0, 0};
#pragma unroll
        for (int m = 0; m < 6; ++m) s += v4f{bitsf(g[2 * m].x), bitsf(g[2 * m].z), bitsf(g[2 * m + 1].x), bitsf(g[2 * m + 1].z)};
        *(v4f*)&L.redV[0][(lane & 3) * 64 + 4 * (lane >> 2)] = s;
        if (arrive_last(&L.cnt[1], 8, lane)) { finish(L, out, id * 64, lane); STAMP(6); }
      }
    }
  } else if (role == 4) {
    v128<KF, PF>(L, rv, rw, out, id * 64, 2816 + (w - 1) * 256, w, lane STPASS);
  } else if (role != 5 && w >= 2) {
    const int vw = w - 2;
    if (role == 2) v256<KA, PN>(L, rv, rw, id, (16 + vw) * 128, w, lane, true, 0, 2, 6, 4 STPASS);
    else v256<KA, PN>(L, rv, rw, 3 * id + (vw >> 1), (20 + (vw & 1)) * 128, w, lane, true, vw >> 1, 2 + (vw & 6), 2, 5 STPASS);
  } else {
    wgbar();
  }
}

}

extern "C" void kernel_launch(void* const* d_in, const int* in_sizes, int n_in, void* d_out, int out_size, void* d_ws,
                              size_t ws_size, hipStream_t stream) {
  qk::quip_kernel<<<256, 512, 0, stream>>>((const unsigned short*)d_in[0], (const int*)d_in[1], (const unsigned short*)d_in[2],
                                          (const unsigned short*)d_in[3], (const unsigned short*)d_in[4],
                                          (const unsigned short*)d_in[5], (unsigned short*)d_out, (unsigned char*)d_ws);
}
```

```cpp
#include <hip/hip_runtime.h>
#include <hip/hip_bf16.h>

namespace qk {

typedef unsigned u32;
typedef unsigned long long u64;
typedef u32 v4u __attribute__((ext_vector_type(4)));
typedef u32 v2u __attribute__((ext_vector_type(2)));
typedef float v4f __attribute__((ext_vector_type(4)));
typedef float v2f __attribute__((ext_vector_type(2)));
typedef short v4s __attribute__((ext_vector_type(4)));
typedef __bf16 v8bf __attribute__((ext_vector_type(8)));
typedef __bf16 v2bf __attribute__((ext_vector_type(2)));
typedef __amdgpu_buffer_rsrc_t rsrc_t;

constexpr int D = 576, F = 4608;
constexpr int VP = F * 2;
constexpr int QP = F * 4;
constexpr int UP = D * 2;

#define KN 4
#define KA 4
#define KF 4
#define K0 2
#define S0 6
#define KF2 5
#define SF2 26
#define PN 28
#define PF 27
#define NEARF 28
#define AUXV 2
#define AUXQ 2
#define AUXU 2
#define S1X4 1
#define QLAG 3
#define CODEPF 1
constexpr int SC1 = 16;

constexpr u32 WS_E = 0;
constexpr u32 WS_ACK = 4096;
#define RX 1
#define RH 1
constexpr u32 XR_STRIDE = 9216 + 4096 + 256;
constexpr u32 H_STRIDE = 73728 + 4096 + 256;
constexpr u32 WS_XR = 8192;
constexpr u32 WS_H = WS_XR + 16 * XR_STRIDE + 4096;
constexpr u32 WS_P = (WS_H + 16 * H_STRIDE + 4095) / 4096 * 4096;
constexpr u32 WS_BYTES = WS_P + 72 * 12288;
constexpr u32 WS_DBG = 4u << 20;
static_assert(WS_BYTES < WS_DBG, "ws");

constexpr int CODE_BYTES = 21504;
constexpr int SPIN_LDS = 1 << 22;
constexpr int SPIN_MEM = 1 << 18;

struct __attribute__((aligned(16))) Lds {
  u64 hflag;
  u32 nearEnd;
  u32 tagw;
  u32 cnt[4];
  float redC[8][256];
  float redV[8][512];
  v2u himg[8][256];
};

#define STAMP(i)
#define STDECL
#define STARG
#define STPASS

template <int N> __device__ __forceinline__ void waitvm() { asm volatile("s_waitcnt vmcnt(%0)" :: "n"(N) : "memory"); }
__device__ __forceinline__ void waitlgkm() { asm volatile("s_waitcnt lgkmcnt(0)" ::: "memory"); }
__device__ __forceinline__ void cbar() { asm volatile("" ::: "memory"); }
__device__ __forceinline__ void pin() { __builtin_amdgcn_sched_barrier(0); }
__device__ __forceinline__ void wgbar() { cbar(); __builtin_amdgcn_s_barrier(); cbar(); }
__device__ __forceinline__ rsrc_t mkrs(const void* p, u32 bytes) { return __builtin_amdgcn_make_buffer_rsrc((void*)p, 0, (int)bytes, 0x00020000); }
__device__ __forceinline__ u32 fresh(u32 o) { asm volatile("" : "+v"(o) :: "memory"); return o; }
__device__ __forceinline__ v4u ldg4(rsrc_t rs, u32 off) { return __builtin_amdgcn_raw_buffer_load_b128(rs, off, 0, SC1); }
__device__ __forceinline__ void stg4(rsrc_t rs, u32 off, v4u v) { __builtin_amdgcn_raw_buffer_store_b128(v, rs, off, 0, SC1); }
__device__ __forceinline__ u32 pkbf(float a, float b) {
  v2f t = {a, b};
  return __builtin_bit_cast(u32, __builtin_convertvector(t, v2bf));
}
__device__ __forceinline__ float bflo(u32 w) { return __builtin_bit_cast(float, w << 16); }
__device__ __forceinline__ float bfhi(u32 w) { return __builtin_bit_cast(float, w & 0xffff0000u); }
__device__ __forceinline__ u32 perm(u32 w, u32 sel) { return __builtin_amdgcn_perm(w, w, sel); }
__device__ __forceinline__ bool tagok(v4u g, u32 tag) { return g.y == tag && g.w == tag; }
__device__ __forceinline__ u32 fbits(float f) { return __builtin_bit_cast(u32, f); }
__device__ __forceinline__ float bitsf(u32 u) { return __builtin_bit_cast(float, u); }

__device__ __forceinline__ u32 hbase() { return WS_H + (blockIdx.x & (RH - 1)) * H_STRIDE; }
struct Sel { u32 aLo, bLo, aHi, bHi; };
__device__ __forceinline__ Sel mksel(int lane) {
  const int s = (lane & 15) >> 2;
  Sel r;
  r.aLo = s == 0 ? 0x0c0c0100u : s == 1 ? 0x01000c0cu : 0x0c0c0c0cu;
  r.bLo = s == 2 ? 0x0c0c0100u : s == 3 ? 0x01000c0cu : 0x0c0c0c0cu;
  r.aHi = s == 0 ? 0x0c0c0302u : s == 1 ? 0x03020c0cu : 0x0c0c0c0cu;
  r.bHi = s == 2 ? 0x0c0c0302u : s == 3 ? 0x03020c0cu : 0x0c0c0c0cu;
  return r;
}
__device__ __forceinline__ v4f mfma16(v2u a, v2u b, v4f c) {
  return __builtin_amdgcn_mfma_f32_16x16x16bf16_1k(__builtin_bit_cast(v4s, a), __builtin_bit_cast(v4s, b), c, 0, 0, 0);
}
__device__ __forceinline__ v4f mfma32(v4u a, v4u b, v4f c) {
  return __builtin_amdgcn_mfma_f32_16x16x32_bf16(__builtin_bit_cast(v8bf, a), __builtin_bit_cast(v8bf, b), c, 0, 0, 0);
}

__device__ __forceinline__ bool arrive_last(u32* cnt, u32 n, int lane) {
  waitlgkm();
  u32 old = 0;
  if (lane == 0) old = __hip_atomic_fetch_add(cnt, 1u, __ATOMIC_RELAXED, __HIP_MEMORY_SCOPE_WORKGROUP);
  old = __builtin_amdgcn_readfirstlane(old);
  cbar();
  return old == n - 1;
}
__device__ __forceinline__ u32 wait_hflag(Lds& L) {
  u64 f = 0;
#pragma nounroll
  for (int it = 0; it < SPIN_LDS; ++it) {
    f = __hip_atomic_load(&L.hflag, __ATOMIC_RELAXED, __HIP_MEMORY_SCOPE_WORKGROUP);
    if (__builtin_amdgcn_readfirstlane((u32)(f >> 32))) break;
    __builtin_amdgcn_s_sleep(1);
  }
  cbar();
  return __builtin_amdgcn_readfirstlane((u32)f);
}
__device__ __forceinline__ void watch_h(Lds& L, rsrc_t rw, u32 tag, int strip0, int nstrip, int lane) {
  const u32 off = hbase() + (strip0 + lane) * 512;
  bool ok = lane != 0;
#pragma nounroll
  for (int it = 0; it < SPIN_MEM; ++it) {
    cbar();
    if (!ok) ok = tagok(ldg4(rw, fresh(off)), tag);
    if (__all(ok)) break;
    __builtin_amdgcn_s_sleep(4);
  }
  ok = lane == 0 || lane >= nstrip;
#pragma nounroll
  for (int it = 0; it < SPIN_MEM; ++it) {
    cbar();
    if (!ok) ok = tagok(ldg4(rw, fresh(off)), tag);
    if (__all(ok)) break;
    __builtin_amdgcn_s_sleep(1);
  }
  cbar();
  if (lane == 0) __hip_atomic_store(&L.hflag, (u64)tag | (1ull << 32), __ATOMIC_RELAXED, __HIP_MEMORY_SCOPE_WORKGROUP);
}

template <int N>
__device__ __forceinline__ void fix_granules(v4u (&g)[N], rsrc_t rw, u32 off, u32 stride, u32 tag) {
  bool ok = true;
#pragma unroll
  for (int j = 0; j < N; ++j) ok = ok && tagok(g[j], tag);
  if (__builtin_expect(!__all(ok), 0)) {
#pragma nounroll
    for (int it = 0; it < SPIN_MEM; ++it) {
      cbar();
      __builtin_amdgcn_s_sleep(1);
      ok = true;
      const u32 o = fresh(off);
#pragma unroll
      for (int j = 0; j < N; ++j) { g[j] = ldg4(rw, o + j * stride); }
#pragma unroll
      for (int j = 0; j < N; ++j) ok = ok && tagok(g[j], tag);
      if (__all(ok)) break;
    }
  }
}

template <int K, int P, int KI, int SI>
__device__ __forceinline__ void v256(Lds& L, rsrc_t rv, rsrc_t rw, int team, int row0, int w, int lane, bool early,
                                     int grp, int gw0, int gn, int member STARG) {
  constexpr int NL = 32;
  const int q = lane >> 4, n = lane & 15, b = lane & 3;
  const u32 voff = (row0 + 4 * q) * VP + team * 256 + n * 16;
  v4u v[NL];
  auto issue = [&](int i) { v[i] = __builtin_amdgcn_raw_buffer_load_b128(rv, voff, (16 * (i >> 2) + (i & 3)) * VP, AUXV); };
  if (!early) wgbar();
#pragma unroll
  for (int i = 0; i < KI; ++i) { issue(i); pin(); }
  if (early) wgbar();
  const Sel sel = mksel(lane);
#pragma unroll
  for (int i = KI; i < P; ++i) { if (i < SI) waitvm<KI - 1>(); else if (i >= K) waitvm<K - 1>(); issue(i); pin(); }
  STAMP(1);
  const u32 tag = wait_hflag(L);
  STAMP(2);
  v4u g[2];
  const u32 hoff = hbase() + row0 * 16 + lane * 16;
  g[0] = ldg4(rw, hoff); g[1] = ldg4(rw, hoff + 1024);
  pin();
  constexpr int P2 = P + K < NL ? P + K : NL;
#pragma unroll
  for (int i = P; i < P2; ++i) { waitvm<K - 1>(); issue(i); pin(); }
  fix_granules<2>(g, rw, hoff, 1024, tag);
  STAMP(3);
  L.himg[w][lane] = v2u{g[0].x, g[0].z};
  L.himg[w][64 + lane] = v2u{g[1].x, g[1].z};
  waitlgkm();
  v2u hq[8];
#pragma unroll
  for (int p = 0; p < 8; ++p) hq[p] = L.himg[w][(4 * p + q) * 4 + b];
  v4f acc0 = {0, 0, 0, 0}, acc1 = {0, 0, 0, 0};
  auto work = [&](int j) {
    const u32 hw = (j & 2) ? hq[j >> 2].y : hq[j >> 2].x;
    const v2u a = (j & 1) ? v2u{perm(hw, sel.aHi), perm(hw, sel.bHi)} : v2u{perm(hw, sel.aLo), perm(hw, sel.bLo)};
    acc0 = mfma16(a, v2u{v[j].x, v[j].y}, acc0);
    acc1 = mfma16(a, v2u{v[j].z, v[j].w}, acc1);
  };
  constexpr int R = NL - P2;
  constexpr int C = R > 0 ? (NL - K + R - 1) / R : 0;
  int done = 0;
#pragma unroll
  for (int i = P2; i < NL; ++i) {
    waitvm<K - 1>(); issue(i); pin();
    const int lim = (i - P2 + 1) * C < i - K + 1 ? (i - P2 + 1) * C : i - K + 1;
#pragma unroll
    for (int j = 0; j < NL; ++j) if (j >= done && j < lim) work(j);
    done = lim;
    pin();
  }
  STAMP(4);
#pragma unroll
  for (int j = 0; j < NL; ++j) if (j >= done) work(j);
  STAMP(5);
  {
    float* dst = &L.redV[w][8 * n + q];
#pragma unroll
    for (int r = 0; r < 4; ++r) { dst[r * 128] = acc0[r]; dst[r * 128 + 4] = acc1[r]; }
  }
  if (arrive_last(&L.cnt[1 + grp], gn, lane)) {
    const int Q = lane >> 2;
    v4f s0 = {0, 0, 0, 0}, s1 = {0, 0, 0, 0};
    for (int k = 0; k < gn; ++k) {
      const float* src = &L.redV[gw0 + k][b * 128 + 4 * Q];
      s0 += *(const v4f*)src;
      s1 += *(const v4f*)(src + 64);
    }
    const u32 po = WS_P + (team * 2) * 12288 + member * 2048 + lane * 16;
    stg4(rw, po, v4u{fbits(s0[0]), tag, fbits(s0[1]), tag});
    stg4(rw, po + 1024, v4u{fbits(s0[2]), tag, fbits(s0[3]), tag});
    stg4(rw, po + 12288, v4u{fbits(s1[0]), tag, fbits(s1[1]), tag});
    stg4(rw, po + 12288 + 1024, v4u{fbits(s1[2]), tag, fbits(s1[3]), tag});
    STAMP(6);
  }
}

__device__ __forceinline__ void finish(Lds& L, unsigned short* out, int col0, int lane) {
  const int Q = lane >> 2, b = lane & 3;
  v4f s = {0, 0, 0, 0};
#pragma unroll
  for (int k = 0; k < 8; ++k) s += *(const v4f*)&L.redV[k][b * 64 + 4 * Q];
  *(__attribute__((address_space(1))) v2u*)(out + b * F + col0 + 4 * Q) = v2u{pkbf(s[0], s[1]), pkbf(s[2], s[3])};
}

template <int K, int P>
__device__ __forceinline__ void v128(Lds& L, rsrc_t rv, rsrc_t rw, unsigned short* out, int col0, int row0, int w, int lane STARG) {
  constexpr int NL = 32;
  const int q = lane >> 4, n = lane & 15, b = lane & 3;
  const u32 voff = (row0 + 4 * q) * VP + col0 * 2 + n * 8;
  v2u v[2 * NL];
  auto issue = [&](int i) {
    const int r = 16 * (i >> 1) + 2 * (i & 1);
    v[2 * i] = __builtin_amdgcn_raw_buffer_load_b64(rv, voff, r * VP, AUXV);
    v[2 * i + 1] = __builtin_amdgcn_raw_buffer_load_b64(rv, voff, (r + 1) * VP, AUXV);
  };
#pragma unroll
  for (int i = 0; i < K0; ++i) { issue(i); pin(); }
  wgbar();
  const Sel sel = mksel(lane);
  auto thr = [&](int i) { if (i < S0) waitvm<2 * K0 - 2>(); else if (i >= SF2) waitvm<2 * KF2 - 2>(); else if (i >= K) waitvm<2 * K - 2>(); };
#pragma unroll
  for (int i = K0; i < P; ++i) { thr(i); issue(i); pin(); }
  STAMP(1);
  const u32 tag = wait_hflag(L);
  STAMP(2);
  v4u g[4];
  const u32 hoff = hbase() + row0 * 16 + lane * 16;
#pragma unroll
  for (int j = 0; j < 4; ++j) g[j] = ldg4(rw, hoff + j * 1024);
  pin();
  constexpr int P2 = P + K < NL ? P + K : NL;
  auto near = [&](int i) { if (i == NEARF && lane == 0) __hip_atomic_store(&L.nearEnd, 1u, __ATOMIC_RELAXED, __HIP_MEMORY_SCOPE_WORKGROUP); };
  static_assert(NEARF >= P && NEARF < NL, "nearf");
#pragma unroll
  for (int i = P; i < P2; ++i) { thr(i); issue(i); near(i); pin(); }
  fix_granules<4>(g, rw, hoff, 1024, tag);
  STAMP(3);
#pragma unroll
  for (int j = 0; j < 4; ++j) L.himg[w][64 * j + lane] = v2u{g[j].x, g[j].z};
  waitlgkm();
  v2u hq[16];
#pragma unroll
  for (int p = 0; p < 16; ++p) hq[p] = L.himg[w][(4 * p + q) * 4 + b];
  v4f acc = {0, 0, 0, 0};
  auto work = [&](int j) {
    const u32 hw = (j & 1) ? hq[j >> 1].y : hq[j >> 1].x;
    const v4u a = {perm(hw, sel.aLo), perm(hw, sel.bLo), perm(hw, sel.aHi), perm(hw, sel.bHi)};
    acc = mfma32(a, v4u{v[2 * j].x, v[2 * j].y, v[2 * j + 1].x, v[2 * j + 1].y}, acc);
  };
  constexpr int R = NL - P2;
  constexpr int C = R > 0 ? (NL - K + R - 1) / R : 0;
  int done = 0;
#pragma unroll
  for (int i = P2; i < NL; ++i) {
    thr(i); issue(i); near(i); pin();
    const int kk = i >= SF2 ? KF2 : K;
    const int lim0 = (i - P2 + 1) * C < i - kk + 1 ? (i - P2 + 1) * C : i - kk + 1;
    const int lim = lim0 > done ? lim0 : done;
#pragma unroll
    for (int j = 0; j < NL; ++j) if (j >= done && j < lim) work(j);
    done = lim;
    pin();
  }
  STAMP(4);
#pragma unroll
  for (int j = 0; j < NL; ++j) if (j >= done) work(j);
  STAMP(5);
  {
    float* dst = &L.redV[w][4 * n + q];
#pragma unroll
    for (int r = 0; r < 4; ++r) dst[r * 64] = acc[r];
  }
  if (arrive_last(&L.cnt[1], 8, lane)) { finish(L, out, col0, lane); STAMP(6); }
}

typedef u32 v16u __attribute__((ext_vector_type(16)));
__device__ __forceinline__ const void* mkptr(u32 lo, u32 hi) { return (const void*)(((u64)hi << 32) | lo); }

__global__ void __launch_bounds__(512, 2)
quip_kernel(const unsigned short* x_, const int* qw_, const unsigned short* sc_, const unsigned short* zr_, const unsigned short* U_,
            const unsigned short* V_, unsigned short* out_, unsigned char* ws_) {
  __shared__ Lds L;
  v16u ka;
  asm volatile("s_load_dwordx16 %0, %1, 0x0" : "=s"(ka) : "s"(__builtin_amdgcn_kernarg_segment_ptr()));
  u64 pc0;
  asm volatile("s_getpc_b64 %0" : "=s"(pc0));
  STDECL;
  STAMP(0);
  const int tid = threadIdx.x, lane = tid & 63;
  const int w = __builtin_amdgcn_readfirstlane(tid >> 6);
  const int wg = blockIdx.x, xc = wg & 7, j = wg >> 3;

  const int r_ = j < 3 ? j * 5 + xc - 3 : wg - 9;
  const bool isF = wg >= 184, isB = wg >= 40, isS1 = j < 3 && xc < 3;
  const int role = isF ? 4 : isB ? 1 : isS1 ? 0 : r_ < 27 ? 2 : r_ < 30 ? 3 : 5;
  const int id = isF ? wg - 184 : isB ? wg - 40 : isS1 ? j * 3 + xc : r_ < 27 ? 9 + r_ : r_ - 27;

  if (w == 0 && lane < 8) ((u32*)&L)[lane] = 0;
  asm volatile("s_waitcnt lgkmcnt(0)" : "+s"(ka) :: "memory");
  const void* x = mkptr(ka[0], ka[1]);
  const void* qw = mkptr(ka[2], ka[3]);
  const void* sc = mkptr(ka[4], ka[5]);
  const void* zr = mkptr(ka[6], ka[7]);
  const void* U = mkptr(ka[8], ka[9]);
  unsigned short* out = (unsigned short*)mkptr(ka[12], ka[13]);
  unsigned char* ws = (unsigned char*)mkptr(ka[14], ka[15]);
  const rsrc_t rw = mkrs(ws, WS_BYTES);
  const rsrc_t rv = mkrs(mkptr(ka[10], ka[11]), F * VP);

  if (role <= 1) {
    if (w >= 4) {
      const int team = role ? id >> 2 : id;
      const int slot = role ? 4 * (id & 3) + (w - 4) : 16 + (w - 4);
      v256<KN, PN, KN, 0>(L, rv, rw, team, slot * 128, w, lane, false, 0, 4, 4, role ? id & 3 : 4 STPASS);
    } else {
      __builtin_amdgcn_s_setprio(3);
      const int q = lane >> 4, n = lane & 15, b = lane & 3;
      u32 E = 0;
      if (w == 0) E = __builtin_amdgcn_raw_buffer_load_b32(rw, WS_E, 0, SC1);
      pin();
      if (role == 0) {
        const rsrc_t rx = mkrs(x, 4 * D * 2), ru = mkrs(U, D * UP);
        v2u X[9];
        const u32 xoff = b * UP + (144 * w + 4 * q) * 2;
        v4u Ut[18];
        const u32 uoff = (144 * w + 4 * q + (n >> 3)) * UP + id * 128 + (n & 7) * 16;
#pragma unroll
        for (int p = 0; p < 9; ++p) {
          X[p] = __builtin_amdgcn_raw_buffer_load_b64(rx, xoff, p * 32, 0);
#pragma unroll
          for (int r = 0; r < 2; ++r) Ut[2 * p + r] = __builtin_amdgcn_raw_buffer_load_b128(ru, uoff, (16 * p + 2 * r) * UP, AUXU);
          pin();
        }
        if (w == 0) { L.tagw = E + 1; waitlgkm(); }
        wgbar();
        STAMP(1);
        const u32 tag = __builtin_amdgcn_readfirstlane(L.tagw);
        if (w == 0 && lane == 0) __builtin_amdgcn_raw_buffer_store_b64(v2u{tag, tag}, rw, WS_ACK + wg * 8, 0, SC1);
        const Sel sel = mksel(lane);
        v4f a00 = {0, 0, 0, 0}, a01 = a00, a10 = a00, a11 = a00;
#pragma unroll
        for (int c = 0; c < 18; ++c) {
          const u32 hw = (c & 1) ? X[c >> 1].y : X[c >> 1].x;
          const v2u lo = {perm(hw, sel.aLo), perm(hw, sel.bLo)}, hi = {perm(hw, sel.aHi), perm(hw, sel.bHi)};
          a00 = mfma16(lo, v2u{Ut[c].x, Ut[c].y}, a00);
          a01 = mfma16(lo, v2u{Ut[c].z, Ut[c].w}, a01);
          a10 = mfma16(hi, v2u{Ut[c].x, Ut[c].y}, a10);
          a11 = mfma16(hi, v2u{Ut[c].z, Ut[c].w}, a11);
        }
        {
          const bool h1 = n >= 8;
          float* dst = &L.redC[2 * w + (n >> 3)][8 * (n & 7) + q];
#pragma unroll
          for (int r = 0; r < 4; ++r) { dst[r * 64] = h1 ? a10[r] : a00[r]; dst[r * 64 + 4] = h1 ? a11[r] : a01[r]; }
        }
        constexpr int NS1 = 8;
        STAMP(2);
        if (arrive_last(&L.cnt[0], 4, lane)) {
          const int Q = lane >> 2;
          v4f s = {0, 0, 0, 0};
#pragma unroll
          for (int k = 0; k < NS1; ++k) s += *(const v4f*)&L.redC[k][b * 64 + 4 * Q];
          const v4u gv = {pkbf(s[0], s[1]), tag, pkbf(s[2], s[3]), tag};
#pragma unroll
          for (int r = 0; r < RX; ++r) __builtin_amdgcn_raw_buffer_store_b128(gv, rw, WS_XR + id * 1024 + lane * 16, r * XR_STRIDE, SC1);
          STAMP(3);
          watch_h(L, rw, tag, 64, 16, lane);
          STAMP(4);
        }
      } else {
        const rsrc_t rq = mkrs(qw, D * QP), rsn = mkrs(sc, 18 * VP), rz = mkrs(zr, 18 * VP);
        v2u Z[9], S[9];
        v4u Q[18];
        const u32 soff = id * 64 + (n & 7) * 8;
        const u32 qoff = (144 * w + 4 * q + (n >> 3)) * QP + id * 128 + (n & 7) * 16;
        u32 W[36];
        auto deq = [&](int p) {
          const float z0 = bflo(Z[p].x), z1 = bfhi(Z[p].x), z2 = bflo(Z[p].y), z3 = bfhi(Z[p].y);
          const float s0 = bflo(S[p].x), s1 = bfhi(S[p].x), s2 = bflo(S[p].y), s3 = bfhi(S[p].y);
#pragma unroll
          for (int r = 0; r < 2; ++r) {
            const v4u t = Q[2 * p + r];
            const u32 a0 = pkbf((float)(int)t.x, (float)(int)t.y), a1 = pkbf((float)(int)t.z, (float)(int)t.w);
            const u32 c0 = pkbf(bflo(a0) - z0, bfhi(a0) - z1), c1 = pkbf(bflo(a1) - z2, bfhi(a1) - z3);
            W[4 * p + 2 * r] = pkbf(bflo(c0) * s0, bfhi(c0) * s1);
            W[4 * p + 2 * r + 1] = pkbf(bflo(c1) * s2, bfhi(c1) * s3);
            asm volatile("" : "+v"(W[4 * p + 2 * r]), "+v"(W[4 * p + 2 * r + 1]));
          }
        };
#pragma unroll
        for (int p = 0; p < 9; ++p) {
          const int grp = (144 * w + 16 * p) >> 5;
          Z[p] = __builtin_amdgcn_raw_buffer_load_b64(rz, soff, grp * VP, 0);
          S[p] = __builtin_amdgcn_raw_buffer_load_b64(rsn, soff, grp * VP, 0);
#pragma unroll
          for (int r = 0; r < 2; ++r) Q[2 * p + r] = __builtin_amdgcn_raw_buffer_load_b128(rq, qoff, (16 * p + 2 * r) * QP, AUXQ);
          pin();
          if (p >= QLAG && p < 8) { deq(p - QLAG); pin(); }
        }
        if (w == 0) { L.tagw = E + 1; waitlgkm(); }
        wgbar();
        STAMP(1);
        const u32 tag = __builtin_amdgcn_readfirstlane(L.tagw);
        if (w == 0 && lane == 0) __builtin_amdgcn_raw_buffer_store_b64(v2u{tag, tag}, rw, WS_ACK + wg * 8, 0, SC1);
#pragma unroll
        for (int p = (8 - QLAG > 0 ? 8 - QLAG : 0); p < 9; ++p) { deq(p); pin(); }
        const Sel sel = mksel(lane);
        STAMP(2);
        v4u g[9];
        const u32 goff = WS_XR + (wg & (RX - 1)) * XR_STRIDE + ((36 * w + q) * 4 + b) * 16;
#pragma nounroll
        for (int it = 0; it < SPIN_MEM; ++it) {
          const u32 o = fresh(goff);
#pragma unroll
          for (int p = 0; p < 9; ++p) g[p] = ldg4(rw, o + p * 256);
          bool ok = true;
#pragma unroll
          for (int p = 0; p < 9; ++p) ok = ok && tagok(g[p], tag);
          if (__all(ok)) break;
          __builtin_amdgcn_s_sleep(2);
        }
        STAMP(4);
        v4f acc0 = {0, 0, 0, 0}, acc1 = acc0;
#pragma unroll
        for (int p = 0; p < 9; ++p) {
          const v4u bw = {W[4 * p], W[4 * p + 1], W[4 * p + 2], W[4 * p + 3]};
          const v4u lo = {perm(g[p].x, sel.aLo), perm(g[p].x, sel.bLo), perm(g[p].z, sel.aLo), perm(g[p].z, sel.bLo)};
          const v4u hi = {perm(g[p].x, sel.aHi), perm(g[p].x, sel.bHi), perm(g[p].z, sel.aHi), perm(g[p].z, sel.bHi)};
          acc0 = mfma32(lo, bw, acc0);
          acc1 = mfma32(hi, bw, acc1);
        }
        {
          const bool h1 = n >= 8;
          float* dst = &L.redC[2 * w + (n >> 3)][4 * (n & 7) + q];
#pragma unroll
          for (int r = 0; r < 4; ++r) dst[r * 32] = h1 ? acc1[r] : acc0[r];
        }
        if (arrive_last(&L.cnt[0], 4, lane)) {
          {
            const int Qd = (lane & 31) >> 2;
            v4f s = {0, 0, 0, 0};
#pragma unroll
            for (int k = 0; k < 8; ++k) s += *(const v4f*)&L.redC[k][b * 32 + 4 * Qd];
            const v4u gv = {pkbf(s[0], s[1]), tag, pkbf(s[2], s[3]), tag};
            if (RH > 1 || lane < 32) {
#pragma unroll
              for (int r = 0; r < (RH + 1) / 2; ++r)
                __builtin_amdgcn_raw_buffer_store_b128(gv, rw, WS_H + id * 512 + (lane & 31) * 16 + (lane >> 5) * H_STRIDE, 2 * r * H_STRIDE, SC1);
            }
          }
          STAMP(5);
          watch_h(L, rw, tag, 16 * (id & 3), 16, lane);
          STAMP(6);
        }
      }
    }
  } else if (w == 0) {
    __builtin_amdgcn_s_setprio(3);
    u32 E = __builtin_amdgcn_raw_buffer_load_b32(rw, WS_E, 0, SC1);
    wgbar();
    const u32 tag = __builtin_amdgcn_readfirstlane(E) + 1;
    if (lane == 0) __builtin_amdgcn_raw_buffer_store_b64(v2u{tag, tag}, rw, WS_ACK + wg * 8, 0, SC1);
    STAMP(1);
    if (role == 5) {
#pragma nounroll
      for (int it = 0; it < SPIN_MEM; ++it) {
        cbar();
        const u32 o = fresh(WS_ACK + lane * 16);
        const v4u a0 = ldg4(rw, o), a1 = ldg4(rw, o + 1024);
        const bool ok = a0.x == tag && a0.y == tag && a0.z == tag && a0.w == tag && a1.x == tag && a1.y == tag && a1.z == tag && a1.w == tag;
        if (__all(ok)) break;
        __builtin_amdgcn_s_sleep(8);
      }
      cbar();
      if (lane == 0) __builtin_amdgcn_raw_buffer_store_b32(tag, rw, WS_E, 0, SC1);
      STAMP(2);
    } else {
      watch_h(L, rw, tag, role == 4 ? 88 : role == 2 ? 64 : 80, role == 4 ? 56 : role == 2 ? 24 : 8, lane);
      STAMP(2);
      if (role == 4) {
      #pragma nounroll
  for (int it = 0; it < SPIN_LDS; ++it) {
          if (__builtin_amdgcn_readfirstlane(__hip_atomic_load(&L.nearEnd, __ATOMIC_RELAXED, __HIP_MEMORY_SCOPE_WORKGROUP))) break;
          __builtin_amdgcn_s_sleep(2);
        }
        cbar();
        STAMP(3);
        const u32 po = WS_P + id * 12288 + lane * 16;
        const bool six = id < 18;
        v4u g[12];
#pragma nounroll
        for (int it = 0; it < SPIN_MEM; ++it) {
          const u32 o = fresh(po);
#pragma unroll
          for (int k = 0; k < 10; ++k) g[k] = ldg4(rw, o + k * 1024);
          if (six) { g[10] = ldg4(rw, o + 10 * 1024); g[11] = ldg4(rw, o + 11 * 1024); }
          else { g[10] = v4u{0, tag, 0, tag}; g[11] = g[10]; }
          bool ok = true;
#pragma unroll
          for (int k = 0; k < 12; ++k) ok = ok && tagok(g[k], tag);
          if (__all(ok)) break;
          __builtin_amdgcn_s_sleep(1);
        }
        STAMP(4);
        v4f s = {0, 0, 0, 0};
#pragma unroll
        for (int m = 0; m < 6; ++m) s += v4f{bitsf(g[2 * m].x), bitsf(g[2 * m].z), bitsf(g[2 * m + 1].x), bitsf(g[2 * m + 1].z)};
        *(v4f*)&L.redV[0][(lane & 3) * 64 + 4 * (lane >> 2)] = s;
        if (arrive_last(&L.cnt[1], 8, lane)) { finish(L, out, id * 64, lane); STAMP(6); }
      }
    }
  } else if (role == 4) {
    v128<KF, PF>(L, rv, rw, out, id * 64, 2816 + (w - 1) * 256, w, lane STPASS);
  } else if (role != 5 && w >= 2) {
    const int vw = w - 2;
    const bool a6 = role == 2;
    v256<KA, PN, K0, S0>(L, rv, rw, a6 ? id : 3 * id + (vw >> 1), (a6 ? 16 + vw : 20 + (vw & 1)) * 128, w, lane, true, a6 ? 0 : vw >> 1,
                 a6 ? 2 : 2 + (vw & 6), a6 ? 6 : 2, a6 ? 4 : 5 STPASS);
  } else {
    if (w == 1) {
      const rsrc_t rc = mkrs((const void*)(pc0 & ~255ull), CODE_BYTES);
      u32 acc = 0;
#pragma unroll
      for (int i = 0; i < (CODE_BYTES + 8191) / 8192; ++i) acc += __builtin_amdgcn_raw_buffer_load_b32(rc, lane * 128 + i * 8192, 0, 0);
      wgbar();
      asm volatile("" :: "v"(acc));
    } else
    wgbar();
  }
}

}

extern "C" void kernel_launch(void* const* d_in, const int* in_sizes, int n_in, void* d_out, int out_size, void* d_ws,
                              size_t ws_size, hipStream_t stream) {
  qk::quip_kernel<<<256, 512, 0, stream>>>((const unsigned short*)d_in[0], (const int*)d_in[1], (const unsigned short*)d_in[2],
                                          (const unsigned short*)d_in[3], (const unsigned short*)d_in[4],
                                          (const unsigned short*)d_in[5], (unsigned short*)d_out, (unsigned char*)d_ws);
}
```

```cpp
#include <hip/hip_runtime.h>
#include <hip/hip_bf16.h>

namespace qk {

typedef unsigned u32;
typedef unsigned long long u64;
typedef u32 v4u __attribute__((ext_vector_type(4)));
typedef u32 v2u __attribute__((ext_vector_type(2)));
typedef float v4f __attribute__((ext_vector_type(4)));
typedef float v2f __attribute__((ext_vector_type(2)));
typedef short v4s __attribute__((ext_vector_type(4)));
typedef __bf16 v8bf __attribute__((ext_vector_type(8)));
typedef __bf16 v2bf __attribute__((ext_vector_type(2)));
typedef __amdgpu_buffer_rsrc_t rsrc_t;

constexpr int D = 576, F = 4608;
constexpr int VP = F * 2;
constexpr int QP = F * 4;
constexpr int UP = D * 2;

#define KN 6
#define KA 4
#define KF 4
#define K0 2
#define S0 6
#define KF2 5
#define SF2 26
#define PN 24
#define PF 22
#define NEARF 27
#define AUXV 2
#define AUXQ 2
#define AUXU 2
#define QLAG 3
#define CODEPF 1
#define MAP 2
#define UPF 2
#define HOLD 0
#define KH 3
#define XST 8
#define SPEC 1
constexpr int SC1 = 16;

constexpr u32 WS_E = 0;
constexpr u32 WS_ACK = 4096;
constexpr u32 WS_XR = 8192;
constexpr u32 WS_H = 20480;
constexpr u32 WS_P = 98304;
constexpr u32 WS_BYTES = WS_P + 72 * 12288;
constexpr u32 WS_DBG = 4u << 20;
static_assert(WS_BYTES < WS_DBG, "ws");

constexpr int CODE_BYTES = 23552;
constexpr int SPIN_LDS = 1 << 22;
constexpr int SPIN_MEM = 1 << 18;

struct __attribute__((aligned(16))) Lds {
  u64 hflag;
  u64 tagv;
  u32 nearEnd;
  u32 issued;
  u32 cnt[4];
  u32 pad[6];
  float redC[4][256];
  float redV[8][512];
  v2u himg[8][256];
  u32 xs[4][288];
};

#define STAMP(i)
#define STDECL
#define STARG
#define STPASS

template <int N> __device__ __forceinline__ void waitvm() { asm volatile("s_waitcnt vmcnt(%0)" :: "n"(N) : "memory"); }
__device__ __forceinline__ void waitlgkm() { asm volatile("s_waitcnt lgkmcnt(0)" ::: "memory"); }
__device__ __forceinline__ void cbar() { asm volatile("" ::: "memory"); }
__device__ __forceinline__ void pin() { __builtin_amdgcn_sched_barrier(0); }
__device__ __forceinline__ void wgbar() { cbar(); __builtin_amdgcn_s_barrier(); cbar(); }
__device__ __forceinline__ rsrc_t mkrs(const void* p, u32 bytes) { return __builtin_amdgcn_make_buffer_rsrc((void*)p, 0, (int)bytes, 0x00020000); }
__device__ __forceinline__ u32 fresh(u32 o) { asm volatile("" : "+v"(o) :: "memory"); return o; }
__device__ __forceinline__ v4u ldg4(rsrc_t rs, u32 off) { return __builtin_amdgcn_raw_buffer_load_b128(rs, off, 0, SC1); }
__device__ __forceinline__ void stg4(rsrc_t rs, u32 off, v4u v) { __builtin_amdgcn_raw_buffer_store_b128(v, rs, off, 0, SC1); }
__device__ __forceinline__ u32 pkbf(float a, float b) {
  v2f t = {a, b};
  return __builtin_bit_cast(u32, __builtin_convertvector(t, v2bf));
}
__device__ __forceinline__ float bflo(u32 w) { return __builtin_bit_cast(float, w << 16); }
__device__ __forceinline__ float bfhi(u32 w) { return __builtin_bit_cast(float, w & 0xffff0000u); }
__device__ __forceinline__ u32 perm(u32 w, u32 sel) { return __builtin_amdgcn_perm(w, w, sel); }
__device__ __forceinline__ bool tagok(v4u g, u32 tag) { return g.y == tag && g.w == tag; }
__device__ __forceinline__ u32 fbits(float f) { return __builtin_bit_cast(u32, f); }
__device__ __forceinline__ float bitsf(u32 u) { return __builtin_bit_cast(float, u); }

struct Sel { u32 aLo, bLo, aHi, bHi; };
__device__ __forceinline__ Sel mksel(int lane) {
  const int s = (lane & 15) >> 2;
  Sel r;
  r.aLo = s == 0 ? 0x0c0c0100u : s == 1 ? 0x01000c0cu : 0x0c0c0c0cu;
  r.bLo = s == 2 ? 0x0c0c0100u : s == 3 ? 0x01000c0cu : 0x0c0c0c0cu;
  r.aHi = s == 0 ? 0x0c0c0302u : s == 1 ? 0x03020c0cu : 0x0c0c0c0cu;
  r.bHi = s == 2 ? 0x0c0c0302u : s == 3 ? 0x03020c0cu : 0x0c0c0c0cu;
  return r;
}
__device__ __forceinline__ v4f mfma16(v2u a, v2u b, v4f c) {
  return __builtin_amdgcn_mfma_f32_16x16x16bf16_1k(__builtin_bit_cast(v4s, a), __builtin_bit_cast(v4s, b), c, 0, 0, 0);
}
__device__ __forceinline__ v4f mfma32(v4u a, v4u b, v4f c) {
  return __builtin_amdgcn_mfma_f32_16x16x32_bf16(__builtin_bit_cast(v8bf, a), __builtin_bit_cast(v8bf, b), c, 0, 0, 0);
}

__device__ __forceinline__ bool arrive_last(u32* cnt, u32 n, int lane) {
  waitlgkm();
  u32 old = 0;
  if (lane == 0) old = __hip_atomic_fetch_add(cnt, 1u, __ATOMIC_RELAXED, __HIP_MEMORY_SCOPE_WORKGROUP);
  old = __builtin_amdgcn_readfirstlane(old);
  cbar();
  return old == n - 1;
}
__device__ __forceinline__ u32 wait_flag(u64* flag) {
  u64 f = 0;
#pragma nounroll
  for (int it = 0; it < SPIN_LDS; ++it) {
    f = __hip_atomic_load(flag, __ATOMIC_RELAXED, __HIP_MEMORY_SCOPE_WORKGROUP);
    if (__builtin_amdgcn_readfirstlane((u32)(f >> 32))) break;
    __builtin_amdgcn_s_sleep(1);
  }
  cbar();
  return __builtin_amdgcn_readfirstlane((u32)f);
}
__device__ __forceinline__ void wait_four(u32* c) {
#pragma nounroll
  for (int it = 0; it < SPIN_LDS; ++it) {
    if (__builtin_amdgcn_readfirstlane(__hip_atomic_load(c, __ATOMIC_RELAXED, __HIP_MEMORY_SCOPE_WORKGROUP)) >= 4) break;
    __builtin_amdgcn_s_sleep(1);
  }
  cbar();
}
__device__ __forceinline__ void watch_h(Lds& L, rsrc_t rw, u32 tag, int strip0, int nstrip, int lane) {
  const u32 off = WS_H + (strip0 + lane) * 512;
  bool ok = lane != 0;
#pragma nounroll
  for (int it = 0; it < SPIN_MEM; ++it) {
    cbar();
    if (!ok) ok = tagok(ldg4(rw, fresh(off)), tag);
    if (__all(ok)) break;
    __builtin_amdgcn_s_sleep(4);
  }
  ok = lane == 0 || lane >= nstrip;
#pragma nounroll
  for (int it = 0; it < SPIN_MEM; ++it) {
    cbar();
    if (!ok) ok = tagok(ldg4(rw, fresh(off)), tag);
    if (__all(ok)) break;
    __builtin_amdgcn_s_sleep(1);
  }
  cbar();
  if (lane == 0) __hip_atomic_store(&L.hflag, (u64)tag | (1ull << 32), __ATOMIC_RELAXED, __HIP_MEMORY_SCOPE_WORKGROUP);
}

template <int N>
__device__ __forceinline__ void fix_granules(v4u (&g)[N], rsrc_t rw, u32 off, u32 stride, u32 tag) {
  bool ok = true;
#pragma unroll
  for (int j = 0; j < N; ++j) ok = ok && tagok(g[j], tag);
  if (__builtin_expect(!__all(ok), 0)) {
#pragma nounroll
    for (int it = 0; it < SPIN_MEM; ++it) {
      cbar();
      __builtin_amdgcn_s_sleep(1);
      ok = true;
      const u32 o = fresh(off);
#pragma unroll
      for (int j = 0; j < N; ++j) { g[j] = ldg4(rw, o + j * stride); }
#pragma unroll
      for (int j = 0; j < N; ++j) ok = ok && tagok(g[j], tag);
      if (__all(ok)) break;
    }
  }
}

template <int K, int P, int KI, int SI, bool CHAIN>
__device__ __forceinline__ void v256(Lds& L, rsrc_t rv, rsrc_t rw, int team, int row0, int w, int lane, bool first,
                                     int grp, int gw0, int gn, int member STARG) {
  constexpr int NL = 32;
  const int q = lane >> 4, n = lane & 15, b = lane & 3;
  const u32 voff = (row0 + 4 * q) * VP + team * 256 + n * 16;
  v4u v[NL];
  auto issue = [&](int i) { v[i] = __builtin_amdgcn_raw_buffer_load_b128(rv, voff, (16 * (i >> 2) + (i & 3)) * VP, AUXV); };
  if (CHAIN) wait_four(first ? &L.cnt[0] : &L.issued);
#pragma unroll
  for (int i = 0; i < KI; ++i) { issue(i); pin(); }
  if (CHAIN) { if (HOLD) wait_four(&L.cnt[0]); STAMP(8); }
  const Sel sel = mksel(lane);
#pragma unroll
  for (int i = KI; i < P; ++i) { if (i < SI) waitvm<KI - 1>(); else if (i >= K) waitvm<K - 1>(); issue(i); pin(); }
  STAMP(1);
  const u32 tag = wait_flag(&L.hflag);
  STAMP(2);
  v4u g[2];
  const u32 hoff = WS_H + row0 * 16 + lane * 16;
  g[0] = ldg4(rw, hoff); g[1] = ldg4(rw, hoff + 1024);
  pin();
  constexpr int P2 = P + K < NL ? P + K : NL;
#pragma unroll
  for (int i = P; i < P2; ++i) { waitvm<K - 1>(); issue(i); pin(); }
  fix_granules<2>(g, rw, hoff, 1024, tag);
  STAMP(3);
  L.himg[w][lane] = v2u{g[0].x, g[0].z};
  L.himg[w][64 + lane] = v2u{g[1].x, g[1].z};
  waitlgkm();
  v2u hq[8];
#pragma unroll
  for (int p = 0; p < 8; ++p) hq[p] = L.himg[w][(4 * p + q) * 4 + b];
  v4f acc0 = {0, 0, 0, 0}, acc1 = {0, 0, 0, 0};
  auto work = [&](int j) {
    const u32 hw = (j & 2) ? hq[j >> 2].y : hq[j >> 2].x;
    const v2u a = (j & 1) ? v2u{perm(hw, sel.aHi), perm(hw, sel.bHi)} : v2u{perm(hw, sel.aLo), perm(hw, sel.bLo)};
    acc0 = mfma16(a, v2u{v[j].x, v[j].y}, acc0);
    acc1 = mfma16(a, v2u{v[j].z, v[j].w}, acc1);
  };
  constexpr int R = NL - P2;
  constexpr int C = R > 0 ? (NL - K + R - 1) / R : 0;
  int done = 0;
#pragma unroll
  for (int i = P2; i < NL; ++i) {
    waitvm<K - 1>(); issue(i); pin();
    const int lim = (i - P2 + 1) * C < i - K + 1 ? (i - P2 + 1) * C : i - K + 1;
#pragma unroll
    for (int j = 0; j < NL; ++j) if (j >= done && j < lim) work(j);
    done = lim;
    pin();
  }
  STAMP(4);
#pragma unroll
  for (int j = 0; j < NL; ++j) if (j >= done) work(j);
  STAMP(5);
  {
    float* dst = &L.redV[w][8 * n + q];
#pragma unroll
    for (int r = 0; r < 4; ++r) { dst[r * 128] = acc0[r]; dst[r * 128 + 4] = acc1[r]; }
  }
  if (arrive_last(&L.cnt[1 + grp], gn, lane)) {
    const int Q = lane >> 2;
    v4f s0 = {0, 0, 0, 0}, s1 = {0, 0, 0, 0};
    for (int k = 0; k < gn; ++k) {
      const float* src = &L.redV[gw0 + k][b * 128 + 4 * Q];
      s0 += *(const v4f*)src;
      s1 += *(const v4f*)(src + 64);
    }
    const u32 po = WS_P + (team * 2) * 12288 + member * 2048 + lane * 16;
    stg4(rw, po, v4u{fbits(s0[0]), tag, fbits(s0[1]), tag});
    stg4(rw, po + 1024, v4u{fbits(s0[2]), tag, fbits(s0[3]), tag});
    stg4(rw, po + 12288, v4u{fbits(s1[0]), tag, fbits(s1[1]), tag});
    stg4(rw, po + 12288 + 1024, v4u{fbits(s1[2]), tag, fbits(s1[3]), tag});
    STAMP(6);
  }
}

__device__ __forceinline__ void finish(Lds& L, unsigned short* out, int col0, int lane) {
  const int Q = lane >> 2, b = lane & 3;
  v4f s = {0, 0, 0, 0};
#pragma unroll
  for (int k = 0; k < 8; ++k) s += *(const v4f*)&L.redV[k][b * 64 + 4 * Q];
  *(__attribute__((address_space(1))) v2u*)(out + b * F + col0 + 4 * Q) = v2u{pkbf(s[0], s[1]), pkbf(s[2], s[3])};
}

template <int K, int P>
__device__ __forceinline__ void v128(Lds& L, rsrc_t rv, rsrc_t rw, unsigned short* out, int col0, int row0, int w, int lane STARG) {
  constexpr int NL = 32;
  const int q = lane >> 4, n = lane & 15, b = lane & 3;
  const u32 voff = (row0 + 4 * q) * VP + col0 * 2 + n * 8;
  v2u v[2 * NL];
  auto issue = [&](int i) {
    const int r = 16 * (i >> 1) + 2 * (i & 1);
    v[2 * i] = __builtin_amdgcn_raw_buffer_load_b64(rv, voff, r * VP, AUXV);
    v[2 * i + 1] = __builtin_amdgcn_raw_buffer_load_b64(rv, voff, (r + 1) * VP, AUXV);
  };
#pragma unroll
  for (int i = 0; i < K0; ++i) { issue(i); pin(); }
  const Sel sel = mksel(lane);
  auto thr = [&](int i) { if (i < S0) waitvm<2 * K0 - 2>(); else if (i >= SF2) waitvm<2 * KF2 - 2>(); else if (i >= K) waitvm<2 * K - 2>(); };
#pragma unroll
  for (int i = K0; i < P; ++i) { thr(i); issue(i); pin(); }
  STAMP(1);
  const u32 tag = wait_flag(&L.hflag);
  STAMP(2);
  v4u g[4];
  const u32 hoff = WS_H + row0 * 16 + lane * 16;
#pragma unroll
  for (int j = 0; j < 4; ++j) g[j] = ldg4(rw, hoff + j * 1024);
  pin();
  constexpr int P2 = P + K < NL ? P + K : NL;
  auto near = [&](int i) { if (i == NEARF && lane == 0) __hip_atomic_store(&L.nearEnd, 1u, __ATOMIC_RELAXED, __HIP_MEMORY_SCOPE_WORKGROUP); };
  static_assert(NEARF >= P && NEARF < NL, "nearf");
#pragma unroll
  for (int i = P; i < P2; ++i) { thr(i); issue(i); near(i); pin(); }
  fix_granules<4>(g, rw, hoff, 1024, tag);
  STAMP(3);
#pragma unroll
  for (int j = 0; j < 4; ++j) L.himg[w][64 * j + lane] = v2u{g[j].x, g[j].z};
  waitlgkm();
  v2u hq[16];
#pragma unroll
  for (int p = 0; p < 16; ++p) hq[p] = L.himg[w][(4 * p + q) * 4 + b];
  v4f acc = {0, 0, 0, 0};
  auto work = [&](int j) {
    const u32 hw = (j & 1) ? hq[j >> 1].y : hq[j >> 1].x;
    const v4u a = {perm(hw, sel.aLo), perm(hw, sel.bLo), perm(hw, sel.aHi), perm(hw, sel.bHi)};
    acc = mfma32(a, v4u{v[2 * j].x, v[2 * j].y, v[2 * j + 1].x, v[2 * j + 1].y}, acc);
  };
  constexpr int R = NL - P2;
  constexpr int C = R > 0 ? (NL - K + R - 1) / R : 0;
  int done = 0;
#pragma unroll
  for (int i = P2; i < NL; ++i) {
    thr(i); issue(i); near(i); pin();
    const int kk = i >= SF2 ? KF2 : K;
    const int lim0 = (i - P2 + 1) * C < i - kk + 1 ? (i - P2 + 1) * C : i - kk + 1;
    const int lim = lim0 > done ? lim0 : done;
#pragma unroll
    for (int j = 0; j < NL; ++j) if (j >= done && j < lim) work(j);
    done = lim;
    pin();
  }
  STAMP(4);
#pragma unroll
  for (int j = 0; j < NL; ++j) if (j >= done) work(j);
  STAMP(5);
  {
    float* dst = &L.redV[w][4 * n + q];
#pragma unroll
    for (int r = 0; r < 4; ++r) dst[r * 64] = acc[r];
  }
  if (arrive_last(&L.cnt[1], 8, lane)) { finish(L, out, col0, lane); STAMP(6); }
}

template <int I> __device__ __forceinline__ const void* karg() {
  u64 p;
  asm volatile("s_mov_b64 %0, s[%1:%2]" : "=s"(p) : "n"(80 + 2 * I), "n"(81 + 2 * I));
  return (const void*)p;
}
#define ARGS() \
  waitlgkm(); \
  const rsrc_t rw = mkrs(karg<7>(), WS_BYTES)
#define VRS() mkrs(karg<5>(), F * VP)
#define OUTP() ((unsigned short*)karg<6>())

__global__ void __launch_bounds__(512, 2) __attribute__((amdgpu_num_sgpr(80)))
quip_kernel(const unsigned short* x_, const int* qw_, const unsigned short* sc_, const unsigned short* zr_, const unsigned short* U_,
            const unsigned short* V_, unsigned short* out_, unsigned char* ws_) {
  __shared__ Lds L;
  STDECL;
  STAMP(0);
  const int tid = threadIdx.x, lane = tid & 63;
  if (tid < 16) { ((u32*)&L)[tid] = 0; waitlgkm(); }
  asm volatile("s_load_dwordx16 s[80:95], %0, 0x0" :: "s"(__builtin_amdgcn_kernarg_segment_ptr())
               : "s80", "s81", "s82", "s83", "s84", "s85", "s86", "s87", "s88", "s89", "s90", "s91", "s92", "s93", "s94", "s95");
  u64 pc0;
  asm volatile("s_getpc_b64 %0" : "=s"(pc0));
  wgbar();
  const int w = __builtin_amdgcn_readfirstlane(tid >> 6);
  const int wg = blockIdx.x, xc = wg & 7, j = wg >> 3;

  int role, id;
  if (xc >= 4 && xc < 7) {
    role = j < 24 ? 4 : 2; id = j < 24 ? 24 * (xc - 4) + j : 8 * xc + j - 47;
  } else if (xc < 3) {
    role = j < 3 ? 0 : j == 3 ? 2 : 1; id = j < 3 ? 3 * j + xc : j == 3 ? 33 + xc : 28 * xc + j - 4;
  } else {
    const int t = 2 * j + (xc >> 2);
    role = t < 3 ? 3 : t == 3 ? 5 : 1; id = t < 4 ? t : 82 + j + 30 * (xc >> 2);
  }
  const int hidx = j - 4;

  if (role <= 1) {
    if (w >= 4) {
      ARGS();
      if (w == 4) {
        const u32 tag = __builtin_amdgcn_readfirstlane(__builtin_amdgcn_raw_buffer_load_b32(rw, WS_E, 0, SC1)) + 1;
        if (lane == 0) {
          __hip_atomic_store(&L.tagv, (u64)tag | (1ull << 32), __ATOMIC_RELAXED, __HIP_MEMORY_SCOPE_WORKGROUP);
          __builtin_amdgcn_raw_buffer_store_b64(v2u{tag, tag}, rw, WS_ACK + wg * 8, 0, SC1);
        }
      }
      const int team = role ? id >> 2 : id;
      const int slot = role ? 4 * (id & 3) + (w - 4) : 16 + (w - 4);
      v256<KN, PN, (HOLD ? KH : KN), 0, true>(L, VRS(), rw, team, slot * 128, w, lane, role == 0, 0, 4, 4, role ? id & 3 : 4 STPASS);
    } else if (role == 0) {
      __builtin_amdgcn_s_setprio(3);
      const int q = lane >> 4, n = lane & 15, b = lane & 3;
      const u32 uoff = (144 * w + 4 * q) * UP + id * 128 + n * 8;
      const u32 xoff = lane * 16 + ((lane * 57) >> 10) * 864;
      ARGS();
      const rsrc_t rx = mkrs(karg<0>(), 4 * D * 2), ru = mkrs(karg<4>(), D * UP);
      const v4u x0 = __builtin_amdgcn_raw_buffer_load_b128(rx, xoff, 288 * w, 0);
      const v4u x1 = __builtin_amdgcn_raw_buffer_load_b128(rx, (lane & 7) * 16 + 3 * UP + 160, 288 * w, 0);
      pin();
      v2u Ut[36], X[9];
      const Sel sel = mksel(lane);
#pragma unroll
      for (int p = 0; p < 9; ++p) {
#pragma unroll
        for (int r = 0; r < 4; ++r) Ut[4 * p + r] = __builtin_amdgcn_raw_buffer_load_b64(ru, uoff, (16 * p + r) * UP, AUXU);
        pin();
        if (p == XST) {
          *(v4u*)&L.xs[w][lane * 4] = x0;
          *(v4u*)&L.xs[w][256 + (lane & 7) * 4] = x1;
          waitlgkm();
#pragma unroll
          for (int k = 0; k < 9; ++k) X[k] = *(const v2u*)&L.xs[w][72 * b + 8 * k + 2 * q];
          pin();
        }
      }
      STAMP(1);
      v4f acc = {0, 0, 0, 0};
      u64 tagf = 0;
#pragma unroll
      for (int c = 0; c < 18; ++c) {
        const u32 hw = (c & 1) ? X[c >> 1].y : X[c >> 1].x;
        const v4u a = {perm(hw, sel.aLo), perm(hw, sel.bLo), perm(hw, sel.aHi), perm(hw, sel.bHi)};
        acc = mfma32(a, v4u{Ut[2 * c].x, Ut[2 * c].y, Ut[2 * c + 1].x, Ut[2 * c + 1].y}, acc);
        if (c == 12) tagf = __hip_atomic_load(&L.tagv, __ATOMIC_RELAXED, __HIP_MEMORY_SCOPE_WORKGROUP);
      }
      {
        float* dst = &L.redC[w][4 * n + q];
#pragma unroll
        for (int r = 0; r < 4; ++r) dst[r * 64] = acc[r];
      }
      STAMP(2);
      if (arrive_last(&L.cnt[0], 4, lane)) {
        const float* src = &L.redC[0][b * 64 + (lane & 60)];
        const v4f s = (*(const v4f*)src + *(const v4f*)(src + 256)) + (*(const v4f*)(src + 512) + *(const v4f*)(src + 768));
        const u32 tag = __builtin_amdgcn_readfirstlane((u32)(tagf >> 32)) ? __builtin_amdgcn_readfirstlane((u32)tagf) : wait_flag(&L.tagv);
        stg4(rw, WS_XR + id * 1024 + lane * 16, v4u{pkbf(s[0], s[1]), tag, pkbf(s[2], s[3]), tag});
        STAMP(3);
        watch_h(L, rw, tag, 64, 16, lane);
        STAMP(4);
      }
    } else {
      __builtin_amdgcn_s_setprio(3);
      const int q = lane >> 4, n = lane & 15, b = lane & 3;
      const u32 soff = id * 64 + (n & 7) * 8;
      const u32 qoff = (144 * w + 4 * q + (n >> 3)) * QP + id * 128 + (n & 7) * 16;
      ARGS();
      v4u pf[UPF];
#pragma unroll
      for (int k = 0; k < UPF; ++k) pf[k] = v4u{0, 0, 0, 0};
      if (xc < 3) {
        const rsrc_t ru = mkrs(karg<4>(), D * UP);
#pragma unroll
        for (int k = 0; k < UPF; ++k) {
          const int t0 = UPF * (4 * hidx + w) + k, t = t0 < 215 ? t0 : 215, v = t / 3, s3 = t - 3 * v;
          pf[k] = __builtin_amdgcn_raw_buffer_load_b128(ru, (lane >> 3) * UP + (lane & 7) * 16,
                                                        (144 * (v & 3) + 136 - 8 * (v >> 2)) * UP + (3 * s3 + xc) * 128, 0);
        }
      }
      pin();
      const rsrc_t rq = mkrs(karg<1>(), D * QP), rsn = mkrs(karg<2>(), 18 * VP), rz = mkrs(karg<3>(), 18 * VP);
      v2u Z[9], S[9];
      v4u Q[18];
      u32 W[36];
      auto deq = [&](int p) {
        const float z0 = bflo(Z[p].x), z1 = bfhi(Z[p].x), z2 = bflo(Z[p].y), z3 = bfhi(Z[p].y);
        const float s0 = bflo(S[p].x), s1 = bfhi(S[p].x), s2 = bflo(S[p].y), s3 = bfhi(S[p].y);
#pragma unroll
        for (int r = 0; r < 2; ++r) {
          const v4u t = Q[2 * p + r];
          const u32 a0 = pkbf((float)(int)t.x, (float)(int)t.y), a1 = pkbf((float)(int)t.z, (float)(int)t.w);
          const u32 c0 = pkbf(bflo(a0) - z0, bfhi(a0) - z1), c1 = pkbf(bflo(a1) - z2, bfhi(a1) - z3);
          W[4 * p + 2 * r] = pkbf(bflo(c0) * s0, bfhi(c0) * s1);
          W[4 * p + 2 * r + 1] = pkbf(bflo(c1) * s2, bfhi(c1) * s3);
          asm volatile("" : "+v"(W[4 * p + 2 * r]), "+v"(W[4 * p + 2 * r + 1]));
        }
      };
#pragma unroll
      for (int p = 0; p < 9; ++p) {
        const int grp = (144 * w + 16 * p) >> 5;
        Z[p] = __builtin_amdgcn_raw_buffer_load_b64(rz, soff, grp * VP, 0);
        S[p] = __builtin_amdgcn_raw_buffer_load_b64(rsn, soff, grp * VP, 0);
#pragma unroll
        for (int r = 0; r < 2; ++r) Q[2 * p + r] = __builtin_amdgcn_raw_buffer_load_b128(rq, qoff, (16 * p + 2 * r) * QP, AUXQ);
        pin();
        if (p >= QLAG && p < 8) { deq(p - QLAG); pin(); }
        if (p == QLAG) {
#pragma unroll
          for (int k = 0; k < UPF; ++k) asm volatile("" :: "v"(pf[k]));
        }
      }
      v4u g[9];
      const u32 goff = WS_XR + ((36 * w + q) * 4 + b) * 16;
#pragma unroll
      for (int p = 0; p < 9; ++p) g[p] = ldg4(rw, goff + p * 256);
      pin();
      if (lane == 0) __hip_atomic_fetch_add(&L.issued, 1u, __ATOMIC_RELAXED, __HIP_MEMORY_SCOPE_WORKGROUP);
      STAMP(1);
#pragma unroll
      for (int p = (8 - QLAG > 0 ? 8 - QLAG : 0); p < 9; ++p) { deq(p); pin(); }
      const Sel sel = mksel(lane);
      const u32 tag = wait_flag(&L.tagv);
      STAMP(2);
      fix_granules<9>(g, rw, goff, 256, tag);
      STAMP(4);
      v4f acc0 = {0, 0, 0, 0}, acc1 = acc0;
#pragma unroll
      for (int p = 0; p < 9; ++p) {
        const v4u bw = {W[4 * p], W[4 * p + 1], W[4 * p + 2], W[4 * p + 3]};
        const v4u lo = {perm(g[p].x, sel.aLo), perm(g[p].x, sel.bLo), perm(g[p].z, sel.aLo), perm(g[p].z, sel.bLo)};
        const v4u hi = {perm(g[p].x, sel.aHi), perm(g[p].x, sel.bHi), perm(g[p].z, sel.aHi), perm(g[p].z, sel.bHi)};
        acc0 = mfma32(lo, bw, acc0);
        acc1 = mfma32(hi, bw, acc1);
      }
      {
        float* dst = &L.redC[w][4 * n + q];
#pragma unroll
        for (int r = 0; r < 4; ++r) {
          const float t = acc0[r] + bitsf(__builtin_amdgcn_update_dpp(0, fbits(acc1[r]), 0x128, 0xf, 0xf, false));
          if (n < 8) dst[r * 32] = t;
        }
      }
      if (arrive_last(&L.cnt[0], 4, lane)) {
        if (lane < 32) {
          const float* src = &L.redC[0][b * 32 + (lane & 28)];
          const v4f s = (*(const v4f*)src + *(const v4f*)(src + 256)) + (*(const v4f*)(src + 512) + *(const v4f*)(src + 768));
          stg4(rw, WS_H + id * 512 + lane * 16, v4u{pkbf(s[0], s[1]), tag, pkbf(s[2], s[3]), tag});
        }
        STAMP(5);
        watch_h(L, rw, tag, 16 * (id & 3), 16, lane);
        STAMP(6);
      }
    }
  } else if (w == 0) {
    __builtin_amdgcn_s_setprio(3);
    ARGS();
    u32 E = __builtin_amdgcn_raw_buffer_load_b32(rw, WS_E, 0, SC1);
    const u32 tag = __builtin_amdgcn_readfirstlane(E) + 1;
    if (lane == 0) __builtin_amdgcn_raw_buffer_store_b64(v2u{tag, tag}, rw, WS_ACK + wg * 8, 0, SC1);
    STAMP(1);
    if (role == 5) {
#pragma nounroll
      for (int it = 0; it < SPIN_MEM; ++it) {
        cbar();
        const u32 o = fresh(WS_ACK + lane * 16);
        const v4u a0 = ldg4(rw, o), a1 = ldg4(rw, o + 1024);
        const bool ok = a0.x == tag && a0.y == tag && a0.z == tag && a0.w == tag && a1.x == tag && a1.y == tag && a1.z == tag && a1.w == tag;
        if (__all(ok)) break;
        __builtin_amdgcn_s_sleep(8);
      }
      cbar();
      if (lane == 0) __builtin_amdgcn_raw_buffer_store_b32(tag, rw, WS_E, 0, SC1);
      STAMP(2);
    } else {
      watch_h(L, rw, tag, role == 4 ? 88 : role == 2 ? 64 : 80, role == 4 ? 56 : role == 2 ? 24 : 8, lane);
      STAMP(2);
      if (role == 4) {
#pragma nounroll
        for (int it = 0; it < SPIN_LDS; ++it) {
          if (__builtin_amdgcn_readfirstlane(__hip_atomic_load(&L.nearEnd, __ATOMIC_RELAXED, __HIP_MEMORY_SCOPE_WORKGROUP))) break;
          __builtin_amdgcn_s_sleep(2);
        }
        cbar();
        STAMP(3);
        const u32 po = WS_P + id * 12288 + lane * 16;
        u32 todo = id < 18 ? 63 : 31;
        v4u g[12];
        g[10] = v4u{0, 0, 0, 0}; g[11] = g[10];
#pragma nounroll
        for (int it = 0; it < SPIN_MEM; ++it) {
          const u32 o = fresh(po);
#pragma unroll
          for (int m = 0; m < 6; ++m)
            if (todo & (1u << m)) { g[2 * m] = ldg4(rw, o + m * 2048); g[2 * m + 1] = ldg4(rw, o + m * 2048 + 1024); }
#pragma unroll
          for (int m = 0; m < 6; ++m)
            if ((todo & (1u << m)) && __all(tagok(g[2 * m], tag) && tagok(g[2 * m + 1], tag))) todo &= ~(1u << m);
          if (!todo) break;
        }
        STAMP(4);
        v4f s = {0, 0, 0, 0};
#pragma unroll
        for (int m = 0; m < 6; ++m) s += v4f{bitsf(g[2 * m].x), bitsf(g[2 * m].z), bitsf(g[2 * m + 1].x), bitsf(g[2 * m + 1].z)};
        *(v4f*)&L.redV[0][(lane & 3) * 64 + 4 * (lane >> 2)] = s;
        if (arrive_last(&L.cnt[1], 8, lane)) { finish(L, OUTP(), id * 64, lane); STAMP(6); }
      }
    }
  } else if (role == 4) {
    ARGS();
    v128<KF, PF>(L, VRS(), rw, OUTP(), id * 64, 2816 + (w - 1) * 256, w, lane STPASS);
  } else if (role != 5 && w >= 2) {
    ARGS();
    const int vw = w - 2;
    const bool a6 = role == 2;
    v256<KA, PN, K0, S0, false>(L, VRS(), rw, a6 ? id : 3 * id + (vw >> 1), (a6 ? 16 + vw : 20 + (vw & 1)) * 128, w, lane, false,
                 a6 ? 0 : vw >> 1, a6 ? 2 : 2 + (vw & 6), a6 ? 6 : 2, a6 ? 4 : 5 STPASS);
  } else {
    if (w == 1) {
      const rsrc_t rc = mkrs((const void*)(pc0 & ~255ull), CODE_BYTES);
      u32 acc = 0;
#pragma unroll
      for (int i = 0; i < (CODE_BYTES + 8191) / 8192; ++i) acc += __builtin_amdgcn_raw_buffer_load_b32(rc, lane * 128 + i * 8192, 0, 0);
      asm volatile("" :: "v"(acc));
    }
  }
}

}

extern "C" void kernel_launch(void* const* d_in, const int* in_sizes, int n_in, void* d_out, int out_size, void* d_ws,
                              size_t ws_size, hipStream_t stream) {
  qk::quip_kernel<<<256, 512, 0, stream>>>((const unsigned short*)d_in[0], (const int*)d_in[1], (const unsigned short*)d_in[2],
                                          (const unsigned short*)d_in[3], (const unsigned short*)d_in[4],
                                          (const unsigned short*)d_in[5], (unsigned short*)d_out, (unsigned char*)d_ws);
}
```

```cpp
#include <hip/hip_runtime.h>
#include <hip/hip_bf16.h>

namespace qk {

typedef unsigned u32;
typedef unsigned long long u64;
typedef u32 v4u __attribute__((ext_vector_type(4)));
typedef u32 v2u __attribute__((ext_vector_type(2)));
typedef float v4f __attribute__((ext_vector_type(4)));
typedef float v2f __attribute__((ext_vector_type(2)));
typedef short v4s __attribute__((ext_vector_type(4)));
typedef __bf16 v8bf __attribute__((ext_vector_type(8)));
typedef __bf16 v2bf __attribute__((ext_vector_type(2)));
typedef __amdgpu_buffer_rsrc_t rsrc_t;

constexpr int D = 576, F = 4608;
constexpr int VP = F * 2;
constexpr int QP = F * 4;
constexpr int UP = D * 2;

#define KN 6
#define KA 4
#define KF 4
#define K0 2
#define S0 6
#define KF2 5
#define SF2 26
#define PN 28
#define PF 26
#define NEARF 27
#define AUXV 2
#define AUXQ 2
#define AUXU 2
#define QLAG 3
#define CODEPF 1
#define MAP 2
#define UPF 2
#define HOLD 0
#define KH 3
#define XST 8
#define SPEC 1
constexpr int SC1 = 16;

constexpr u32 WS_E = 0;
constexpr u32 WS_ACK = 4096;
constexpr u32 WS_XR = 8192;
constexpr u32 WS_H = 20480;
constexpr u32 WS_P = 98304;
constexpr u32 WS_BYTES = WS_P + 72 * 12288;
constexpr u32 WS_DBG = 4u << 20;
static_assert(WS_BYTES < WS_DBG, "ws");

constexpr int CODE_BYTES = 26112;
constexpr int SPIN_LDS = 1 << 22;
constexpr int SPIN_MEM = 1 << 18;
#define GATH 0
#define XTRA 1
#define NPF 40
#define SPECP 8

struct __attribute__((aligned(16))) Lds {
  u64 hflag;
  u64 tagv;
  u32 nearEnd;
  u32 issued;
  u32 cnt[4];
  u32 pad[6];
  float redC[4][256];
  float redV[8][512];
  v2u himg[8][256];
  u32 xs[4][288];
};

#define STAMP(i)
#define STDECL
#define STARG
#define STPASS

template <int N> __device__ __forceinline__ void waitvm() { asm volatile("s_waitcnt vmcnt(%0)" :: "n"(N) : "memory"); }
__device__ __forceinline__ void waitlgkm() { asm volatile("s_waitcnt lgkmcnt(0)" ::: "memory"); }
__device__ __forceinline__ void cbar() { asm volatile("" ::: "memory"); }
__device__ __forceinline__ void pin() { __builtin_amdgcn_sched_barrier(0); }
__device__ __forceinline__ void wgbar() { cbar(); __builtin_amdgcn_s_barrier(); cbar(); }
__device__ __forceinline__ rsrc_t mkrs(const void* p, u32 bytes) { return __builtin_amdgcn_make_buffer_rsrc((void*)p, 0, (int)bytes, 0x00020000); }
__device__ __forceinline__ u32 fresh(u32 o) { asm volatile("" : "+v"(o) :: "memory"); return o; }
__device__ __forceinline__ v4u ldg4(rsrc_t rs, u32 off) { return __builtin_amdgcn_raw_buffer_load_b128(rs, off, 0, SC1); }
__device__ __forceinline__ v4u ldg4s(rsrc_t rs, u32 off, u32 so) { return __builtin_amdgcn_raw_buffer_load_b128(rs, off, so, SC1); }
__device__ __forceinline__ void stg4(rsrc_t rs, u32 off, v4u v) { __builtin_amdgcn_raw_buffer_store_b128(v, rs, off, 0, SC1); }
__device__ __forceinline__ u32 pkbf(float a, float b) {
  v2f t = {a, b};
  return __builtin_bit_cast(u32, __builtin_convertvector(t, v2bf));
}
__device__ __forceinline__ float bflo(u32 w) { return __builtin_bit_cast(float, w << 16); }
__device__ __forceinline__ float bfhi(u32 w) { return __builtin_bit_cast(float, w & 0xffff0000u); }
__device__ __forceinline__ u32 perm(u32 w, u32 sel) { return __builtin_amdgcn_perm(w, w, sel); }
__device__ __forceinline__ bool tagok(v4u g, u32 tag) { return g.y == tag && g.w == tag; }
__device__ __forceinline__ u32 fbits(float f) { return __builtin_bit_cast(u32, f); }
__device__ __forceinline__ float bitsf(u32 u) { return __builtin_bit_cast(float, u); }

struct Sel { u32 aLo, bLo, aHi, bHi; };
__device__ __forceinline__ Sel mksel(int lane) {
  const int s = (lane & 15) >> 2;
  Sel r;
  r.aLo = s == 0 ? 0x0c0c0100u : s == 1 ? 0x01000c0cu : 0x0c0c0c0cu;
  r.bLo = s == 2 ? 0x0c0c0100u : s == 3 ? 0x01000c0cu : 0x0c0c0c0cu;
  r.aHi = s == 0 ? 0x0c0c0302u : s == 1 ? 0x03020c0cu : 0x0c0c0c0cu;
  r.bHi = s == 2 ? 0x0c0c0302u : s == 3 ? 0x03020c0cu : 0x0c0c0c0cu;
  return r;
}
__device__ __forceinline__ v4f mfma16(v2u a, v2u b, v4f c) {
  return __builtin_amdgcn_mfma_f32_16x16x16bf16_1k(__builtin_bit_cast(v4s, a), __builtin_bit_cast(v4s, b), c, 0, 0, 0);
}
__device__ __forceinline__ v4f mfma32(v4u a, v4u b, v4f c) {
  return __builtin_amdgcn_mfma_f32_16x16x32_bf16(__builtin_bit_cast(v8bf, a), __builtin_bit_cast(v8bf, b), c, 0, 0, 0);
}

__device__ __forceinline__ bool arrive_last(u32* cnt, u32 n, int lane) {
  waitlgkm();
  u32 old = 0;
  if (lane == 0) old = __hip_atomic_fetch_add(cnt, 1u, __ATOMIC_RELAXED, __HIP_MEMORY_SCOPE_WORKGROUP);
  old = __builtin_amdgcn_readfirstlane(old);
  cbar();
  return old == n - 1;
}
__device__ __forceinline__ u32 wait_flag(u64* flag) {
  u64 f = 0;
#pragma nounroll
  for (int it = 0; it < SPIN_LDS; ++it) {
    f = __hip_atomic_load(flag, __ATOMIC_RELAXED, __HIP_MEMORY_SCOPE_WORKGROUP);
    if (__builtin_amdgcn_readfirstlane((u32)(f >> 32))) break;
    __builtin_amdgcn_s_sleep(1);
  }
  cbar();
  return __builtin_amdgcn_readfirstlane((u32)f);
}
__device__ __forceinline__ void wait_four(u32* c) {
#pragma nounroll
  for (int it = 0; it < SPIN_LDS; ++it) {
    if (__builtin_amdgcn_readfirstlane(__hip_atomic_load(c, __ATOMIC_RELAXED, __HIP_MEMORY_SCOPE_WORKGROUP)) >= 4) break;
    __builtin_amdgcn_s_sleep(1);
  }
  cbar();
}
template <int NB>
__device__ __forceinline__ void watch_h(Lds& L, rsrc_t rw, u32 tag, int strip0, int nstrip, bool gath, int lane) {
  const u32 off = WS_H + (strip0 + lane) * 512;
  bool ok = lane != 0;
#pragma nounroll
  for (int it = 0; it < SPIN_MEM; ++it) {
    cbar();
    if (!ok) ok = tagok(ldg4(rw, fresh(off)), tag);
    if (__all(ok)) break;
    __builtin_amdgcn_s_sleep(4);
  }
  if (GATH && gath) {
    u32 todo = (1u << (nstrip >> 1)) - 1;
    v4u g[NB];
    v2u* img = &L.himg[4][lane];
#pragma nounroll
    for (int it = 0; it < SPIN_MEM; ++it) {
      const u32 o = fresh(WS_H + strip0 * 512 + lane * 16);
#pragma unroll
      for (int m = 0; m < NB; ++m) if (todo & (1u << m)) g[m] = ldg4s(rw, o, m * 1024);
      waitvm<0>();
#pragma unroll
      for (int m = 0; m < NB; ++m)
        if ((todo & (1u << m)) && __all(tagok(g[m], tag))) { img[m * 64] = v2u{g[m].x, g[m].z}; todo &= ~(1u << m); }
      if (!todo) break;
    }
    waitlgkm();
    nstrip = 0;
  }
  ok = lane == 0 || lane >= nstrip;
#pragma nounroll
  for (int it = 0; it < SPIN_MEM; ++it) {
    cbar();
    if (!ok) ok = tagok(ldg4(rw, fresh(off)), tag);
    if (__all(ok)) break;
    __builtin_amdgcn_s_sleep(1);
  }
  cbar();
  if (lane == 0) __hip_atomic_store(&L.hflag, (u64)tag | (1ull << 32), __ATOMIC_RELAXED, __HIP_MEMORY_SCOPE_WORKGROUP);
}

template <int N>
__device__ __forceinline__ void fix_granules(v4u (&g)[N], rsrc_t rw, u32 off, u32 stride, u32 tag) {
  bool ok = true;
#pragma unroll
  for (int j = 0; j < N; ++j) ok = ok && tagok(g[j], tag);
  if (__builtin_expect(!__all(ok), 0)) {
#pragma nounroll
    for (int it = 0; it < SPIN_MEM; ++it) {
      cbar();
      __builtin_amdgcn_s_sleep(1);
      ok = true;
      const u32 o = fresh(off);
#pragma unroll
      for (int j = 0; j < N; ++j) { g[j] = ldg4(rw, o + j * stride); }
#pragma unroll
      for (int j = 0; j < N; ++j) ok = ok && tagok(g[j], tag);
      if (__all(ok)) break;
    }
  }
}

template <int GM>
__device__ __forceinline__ void publish(Lds& L, rsrc_t rw, int team, int member, int gw0, int gn, bool ex, u32 tag, int lane) {
  const float* src = &L.redV[gw0][(lane & 3) * 128 + (lane & 60)];
  v4f t0[GM], t1[GM], tx = {0, 0, 0, 0};
#pragma unroll
  for (int k = 0; k < GM; ++k) {
    const float* p = src + (k < gn ? k : 0) * 512;
    t0[k] = *(const v4f*)p;
    t1[k] = *(const v4f*)(p + 64);
  }
  if (GM > 4) tx = *(const v4f*)&L.redV[1][(lane & 3) * 64 + (lane & 60)];
  v4f s0 = t0[0], s1 = t1[0];
  const v4f zz = {0, 0, 0, 0};
#pragma unroll
  for (int k = 1; k < GM; ++k) { s0 += k < gn ? t0[k] : zz; s1 += k < gn ? t1[k] : zz; }
  if (GM > 4) s1 += ex ? tx : zz;
  const u32 po = WS_P + (team * 2) * 12288 + member * 2048 + lane * 16;
  stg4(rw, po, v4u{fbits(s0[0]), tag, fbits(s0[1]), tag});
  stg4(rw, po + 1024, v4u{fbits(s0[2]), tag, fbits(s0[3]), tag});
  stg4(rw, po + 12288, v4u{fbits(s1[0]), tag, fbits(s1[1]), tag});
  stg4(rw, po + 12288 + 1024, v4u{fbits(s1[2]), tag, fbits(s1[3]), tag});
}

template <int K, int P, int KI, int SI, bool CHAIN>
__device__ __forceinline__ void v256(Lds& L, rsrc_t rv, rsrc_t rw, int team, int row0, int hs, int w, int lane, bool first,
                                     int grp, int gw0, int gn, bool ex, int member STARG) {
  constexpr int NL = 32;
  const int q = lane >> 4, n = lane & 15, b = lane & 3;
  const u32 voff = (row0 + 4 * q) * VP + team * 256 + n * 8;
  v2u v[2 * NL];
  auto issue = [&](int i) {
    v[2 * i] = __builtin_amdgcn_raw_buffer_load_b64(rv, voff, (16 * (i >> 2) + (i & 3)) * VP, AUXV);
    v[2 * i + 1] = __builtin_amdgcn_raw_buffer_load_b64(rv, voff + 128, (16 * (i >> 2) + (i & 3)) * VP, AUXV);
  };
  if (CHAIN) wait_four(first ? &L.cnt[0] : &L.issued);
#pragma unroll
  for (int i = 0; i < KI; ++i) { issue(i); pin(); }
  if (CHAIN) { if (HOLD) wait_four(&L.cnt[0]); STAMP(8); }
  const Sel sel = mksel(lane);
#pragma unroll
  for (int i = KI; i < P; ++i) { if (i < SI) waitvm<2 * KI - 2>(); else if (i >= K) waitvm<2 * K - 2>(); issue(i); pin(); }
  STAMP(1);
  const u32 tag = wait_flag(&L.hflag);
  STAMP(2);
  v2u hq[8];
  v4u g[2];
  const u32 hoff = WS_H + row0 * 16 + lane * 16;
  g[0] = ldg4(rw, hoff); g[1] = ldg4(rw, hoff + 1024);
  pin();
  constexpr int P2 = P + K < NL ? P + K : NL;
#pragma unroll
  for (int i = P; i < P2; ++i) { waitvm<2 * K - 2>(); issue(i); pin(); }
  fix_granules<2>(g, rw, hoff, 1024, tag);
  L.himg[w][lane] = v2u{g[0].x, g[0].z};
  L.himg[w][64 + lane] = v2u{g[1].x, g[1].z};
  waitlgkm();
#pragma unroll
  for (int p = 0; p < 8; ++p) hq[p] = L.himg[w][(4 * p + q) * 4 + b];
  STAMP(3);
  v4f acc0 = {0, 0, 0, 0}, acc1 = {0, 0, 0, 0};
  auto work = [&](int j) {
    const u32 hw = (j & 1) ? hq[j >> 1].y : hq[j >> 1].x;
    const v4u a = {perm(hw, sel.aLo), perm(hw, sel.bLo), perm(hw, sel.aHi), perm(hw, sel.bHi)};
    acc0 = mfma32(a, v4u{v[4 * j].x, v[4 * j].y, v[4 * j + 2].x, v[4 * j + 2].y}, acc0);
    acc1 = mfma32(a, v4u{v[4 * j + 1].x, v[4 * j + 1].y, v[4 * j + 3].x, v[4 * j + 3].y}, acc1);
  };
  constexpr int P3 = GATH ? (P2 + K - 1 < NL ? P2 + K - 1 : NL) : P2;
#pragma unroll
  for (int i = P2; i < P3; ++i) { waitvm<2 * K - 2>(); issue(i); pin(); }
  constexpr int R = NL - P3;
  constexpr int C = R > 0 ? ((NL - K) / 2 + R - 1) / R : 0;
  int done = 0;
#pragma unroll
  for (int i = P3; i < NL; ++i) {
    const int lim = (i - P3 + 1) * C < (i - K) / 2 ? (i - P3 + 1) * C : (i - K) / 2;
#pragma unroll
    for (int j = 0; j < NL / 2; ++j) if (j >= done && j < lim) work(j);
    done = lim;
    pin();
    waitvm<2 * K - 2>(); issue(i); pin();
  }
  STAMP(4);
#pragma unroll
  for (int j = 0; j < NL / 2; ++j) if (j >= done) work(j);
  STAMP(5);
  {
    float* dst = &L.redV[w][4 * n + q];
#pragma unroll
    for (int r = 0; r < 4; ++r) { dst[r * 128] = acc0[r]; dst[r * 128 + 64] = acc1[r]; }
  }
  if (arrive_last(&L.cnt[1 + grp], gn + ex, lane)) {
    publish<CHAIN ? 4 : 6>(L, rw, team, member, gw0, gn, ex, tag, lane);
    STAMP(6);
  }
}

__device__ __forceinline__ void finish(Lds& L, unsigned short* out, int col0, int lane) {
  const int Q = lane >> 2, b = lane & 3;
  v4f s = {0, 0, 0, 0};
#pragma unroll
  for (int k = 0; k < 8; ++k) s += *(const v4f*)&L.redV[k][b * 64 + 4 * Q];
  *(__attribute__((address_space(1))) v2u*)(out + b * F + col0 + 4 * Q) = v2u{pkbf(s[0], s[1]), pkbf(s[2], s[3])};
}

template <int K, int P>
__device__ __forceinline__ void v128(Lds& L, rsrc_t rv, rsrc_t rw, unsigned short* out, int col0, int row0, int w, int lane,
                                     int xg, int xw0, int xn, int xm STARG) {
  constexpr int NL = 32;
  const int q = lane >> 4, n = lane & 15, b = lane & 3;
  const u32 voff = (row0 + 4 * q) * VP + col0 * 2 + n * 8;
  v2u v[2 * NL];
  auto issue = [&](int i) {
    const int r = 16 * (i >> 1) + 2 * (i & 1);
    v[2 * i] = __builtin_amdgcn_raw_buffer_load_b64(rv, voff, r * VP, AUXV);
    v[2 * i + 1] = __builtin_amdgcn_raw_buffer_load_b64(rv, voff, (r + 1) * VP, AUXV);
  };
#pragma unroll
  for (int i = 0; i < K0; ++i) { issue(i); pin(); }
  const Sel sel = mksel(lane);
  auto thr = [&](int i) { if (i < S0) waitvm<2 * K0 - 2>(); else if (i >= SF2) waitvm<2 * KF2 - 2>(); else if (i >= K) waitvm<2 * K - 2>(); };
#pragma unroll
  for (int i = K0; i < P; ++i) { thr(i); issue(i); pin(); }
  STAMP(1);
  const u32 tag = wait_flag(&L.hflag);
  STAMP(2);
  v4u g[4];
  const u32 hoff = WS_H + row0 * 16 + lane * 16;
#pragma unroll
  for (int j = 0; j < 4; ++j) g[j] = ldg4(rw, hoff + j * 1024);
  pin();
  constexpr int P2 = P + K < NL ? P + K : NL;
  auto near = [&](int i) { if (i == NEARF && lane == 0) __hip_atomic_store(&L.nearEnd, 1u, __ATOMIC_RELAXED, __HIP_MEMORY_SCOPE_WORKGROUP); };
  static_assert(NEARF >= P && NEARF < NL, "nearf");
#pragma unroll
  for (int i = P; i < P2; ++i) { thr(i); issue(i); near(i); pin(); }
  fix_granules<4>(g, rw, hoff, 1024, tag);
  STAMP(3);
#pragma unroll
  for (int j = 0; j < 4; ++j) L.himg[w][64 * j + lane] = v2u{g[j].x, g[j].z};
  waitlgkm();
  v2u hq[16];
#pragma unroll
  for (int p = 0; p < 16; ++p) hq[p] = L.himg[w][(4 * p + q) * 4 + b];
  v4f acc = {0, 0, 0, 0};
  auto work = [&](int j) {
    const u32 hw = (j & 1) ? hq[j >> 1].y : hq[j >> 1].x;
    const v4u a = {perm(hw, sel.aLo), perm(hw, sel.bLo), perm(hw, sel.aHi), perm(hw, sel.bHi)};
    acc = mfma32(a, v4u{v[2 * j].x, v[2 * j].y, v[2 * j + 1].x, v[2 * j + 1].y}, acc);
  };
  constexpr int R = NL - P2;
  constexpr int C = R > 0 ? (NL - K + R - 1) / R : 0;
  int done = 0;
#pragma unroll
  for (int i = P2; i < NL; ++i) {
    thr(i); issue(i); near(i); pin();
    const int kk = i >= SF2 ? KF2 : K;
    const int lim0 = (i - P2 + 1) * C < i - kk + 1 ? (i - P2 + 1) * C : i - kk + 1;
    const int lim = lim0 > done ? lim0 : done;
#pragma unroll
    for (int j = 0; j < NL; ++j) if (j >= done && j < lim) work(j);
    done = lim;
    pin();
  }
  STAMP(4);
#pragma unroll
  for (int j = 0; j < NL; ++j) if (j >= done) work(j);
  STAMP(5);
  {
    float* dst = &L.redV[w][4 * n + q];
#pragma unroll
    for (int r = 0; r < 4; ++r) dst[r * 64] = acc[r];
  }
  if (XTRA && xn) {
    if (arrive_last(&L.cnt[1 + xg], xn + 1, lane)) publish<6>(L, rw, col0 >> 7, xm, xw0, xn, true, tag, lane);
  } else if (arrive_last(&L.cnt[1], 8, lane)) { finish(L, out, col0, lane); STAMP(6); }
}

template <int I> __device__ __forceinline__ const void* karg() {
  u64 p;
  asm volatile("s_mov_b64 %0, s[%1:%2]" : "=s"(p) : "n"(80 + 2 * I), "n"(81 + 2 * I));
  return (const void*)p;
}
#define ARGS() \
  waitlgkm(); \
  const rsrc_t rw = mkrs(karg<7>(), WS_BYTES)
#define VRS() mkrs(karg<5>(), F * VP)
#define OUTP() ((unsigned short*)karg<6>())

__global__ void __launch_bounds__(512, 2) __attribute__((amdgpu_num_sgpr(80)))
quip_kernel(const unsigned short* x_, const int* qw_, const unsigned short* sc_, const unsigned short* zr_, const unsigned short* U_,
            const unsigned short* V_, unsigned short* out_, unsigned char* ws_) {
  __shared__ Lds L;
  STDECL;
  STAMP(0);
  const int tid = threadIdx.x, lane = tid & 63;
  if (tid < 16) { ((u32*)&L)[tid] = 0; waitlgkm(); }
  asm volatile("s_load_dwordx16 s[80:95], %0, 0x0" :: "s"(__builtin_amdgcn_kernarg_segment_ptr())
               : "s80", "s81", "s82", "s83", "s84", "s85", "s86", "s87", "s88", "s89", "s90", "s91", "s92", "s93", "s94", "s95");
  u64 pc0;
  asm volatile("s_getpc_b64 %0" : "=s"(pc0));
  wgbar();
  const int w = __builtin_amdgcn_readfirstlane(tid >> 6);
  const int wg = blockIdx.x, xc = wg & 7, j = wg >> 3;

  int role, id;
  if (xc >= 4 && xc < 7) {
    role = j < 24 ? 4 : 2; id = j < 24 ? 24 * (xc - 4) + j : 8 * xc + j - 47;
  } else if (xc < 3) {
    role = j < 3 ? 0 : j == 3 ? 2 : 1; id = j < 3 ? 3 * j + xc : j == 3 ? 33 + xc : 28 * xc + j - 4;
  } else {
    const int t = 2 * j + (xc >> 2);
    role = t < 3 ? 3 : t == 3 ? 5 : 1; id = t < 4 ? t : 82 + j + 30 * (xc >> 2);
  }
  const int hidx = j - 4;

  if (role <= 1) {
    if (w >= 4) {
      ARGS();
      if (w == 4) {
        const u32 tag = __builtin_amdgcn_readfirstlane(__builtin_amdgcn_raw_buffer_load_b32(rw, WS_E, 0, SC1)) + 1;
        if (lane == 0) {
          __hip_atomic_store(&L.tagv, (u64)tag | (1ull << 32), __ATOMIC_RELAXED, __HIP_MEMORY_SCOPE_WORKGROUP);
          __builtin_amdgcn_raw_buffer_store_b64(v2u{tag, tag}, rw, WS_ACK + wg * 8, 0, SC1);
        }
      }
      if (w == 5 && role && (id & 7) != 3) {
        const int r = id & 7, t = (r < 3 ? r : r - 1) + 7 * lane;
        u32 keep = 0;
        if (lane < NPF) keep = __builtin_amdgcn_raw_buffer_load_b32(mkrs(karg<1>(), D * QP), (144 * (t & 3) + 143 - (t >> 2)) * QP + ((id & ~7) + 3) * 128, 0, 0);
        wait_four(&L.issued);
        asm volatile("" :: "v"(keep));
      }
      const int team = role ? id >> 2 : id;
      const int slot = role ? 4 * (id & 3) + (w - 4) : 16 + (w - 4);
      v256<KN, PN, (HOLD ? KH : KN), 0, true>(L, VRS(), rw, team, slot * 128, w - 4, w, lane, role == 0, 0, 4, 4, false, role ? id & 3 : 4 STPASS);
    } else if (role == 0) {
      __builtin_amdgcn_s_setprio(3);
      const int q = lane >> 4, n = lane & 15, b = lane & 3;
      const u32 uoff = (144 * w + 4 * q) * UP + id * 128 + n * 8;
      const u32 xoff = lane * 16 + ((lane * 57) >> 10) * 864;
      ARGS();
      const rsrc_t rx = mkrs(karg<0>(), 4 * D * 2), ru = mkrs(karg<4>(), D * UP);
      const v4u x0 = __builtin_amdgcn_raw_buffer_load_b128(rx, xoff, 288 * w, 0);
      const v4u x1 = __builtin_amdgcn_raw_buffer_load_b128(rx, (lane & 7) * 16 + 3 * UP + 160, 288 * w, 0);
      pin();
      v2u Ut[36], X[9];
      const Sel sel = mksel(lane);
#pragma unroll
      for (int p = 0; p < 9; ++p) {
#pragma unroll
        for (int r = 0; r < 4; ++r) Ut[4 * p + r] = __builtin_amdgcn_raw_buffer_load_b64(ru, uoff, (16 * p + r) * UP, AUXU);
        pin();
        if (p == XST) {
          *(v4u*)&L.xs[w][lane * 4] = x0;
          *(v4u*)&L.xs[w][256 + (lane & 7) * 4] = x1;
          waitlgkm();
#pragma unroll
          for (int k = 0; k < 9; ++k) X[k] = *(const v2u*)&L.xs[w][72 * b + 8 * k + 2 * q];
          pin();
        }
      }
      STAMP(1);
      v4f acc = {0, 0, 0, 0};
      u64 tagf = 0;
#pragma unroll
      for (int c = 0; c < 18; ++c) {
        const u32 hw = (c & 1) ? X[c >> 1].y : X[c >> 1].x;
        const v4u a = {perm(hw, sel.aLo), perm(hw, sel.bLo), perm(hw, sel.aHi), perm(hw, sel.bHi)};
        acc = mfma32(a, v4u{Ut[2 * c].x, Ut[2 * c].y, Ut[2 * c + 1].x, Ut[2 * c + 1].y}, acc);
        if (c == 12) tagf = __hip_atomic_load(&L.tagv, __ATOMIC_RELAXED, __HIP_MEMORY_SCOPE_WORKGROUP);
      }
      {
        float* dst = &L.redC[w][4 * n + q];
#pragma unroll
        for (int r = 0; r < 4; ++r) dst[r * 64] = acc[r];
      }
      STAMP(2);
      if (arrive_last(&L.cnt[0], 4, lane)) {
        const float* src = &L.redC[0][b * 64 + (lane & 60)];
        const v4f s = (*(const v4f*)src + *(const v4f*)(src + 256)) + (*(const v4f*)(src + 512) + *(const v4f*)(src + 768));
        const u32 tag = __builtin_amdgcn_readfirstlane((u32)(tagf >> 32)) ? __builtin_amdgcn_readfirstlane((u32)tagf) : wait_flag(&L.tagv);
        stg4(rw, WS_XR + id * 1024 + lane * 16, v4u{pkbf(s[0], s[1]), tag, pkbf(s[2], s[3]), tag});
        STAMP(3);
        watch_h<8>(L, rw, tag, 64, 16, true, lane);
        STAMP(4);
      }
    } else {
      __builtin_amdgcn_s_setprio(3);
      const int q = lane >> 4, n = lane & 15, b = lane & 3;
      const u32 soff = id * 64 + (n & 7) * 8;
      const u32 qoff = (144 * w + 4 * q + (n >> 3)) * QP + id * 128 + (n & 7) * 16;
      ARGS();
      v4u pf[UPF];
#pragma unroll
      for (int k = 0; k < UPF; ++k) pf[k] = v4u{0, 0, 0, 0};
      if (xc < 3) {
        const rsrc_t ru = mkrs(karg<4>(), D * UP);
#pragma unroll
        for (int k = 0; k < UPF; ++k) {
          const int t0 = UPF * (4 * hidx + w) + k, t = t0 < 215 ? t0 : 215, v = t / 3, s3 = t - 3 * v;
          pf[k] = __builtin_amdgcn_raw_buffer_load_b128(ru, (lane >> 3) * UP + (lane & 7) * 16,
                                                        (144 * (v & 3) + 136 - 8 * (v >> 2)) * UP + (3 * s3 + xc) * 128, 0);
        }
      }
      pin();
      const rsrc_t rq = mkrs(karg<1>(), D * QP), rsn = mkrs(karg<2>(), 18 * VP), rz = mkrs(karg<3>(), 18 * VP);
      v2u Z[9], S[9];
      v4u Q[18];
      u32 W[36];
      auto deq = [&](int p) {
        const float z0 = bflo(Z[p].x), z1 = bfhi(Z[p].x), z2 = bflo(Z[p].y), z3 = bfhi(Z[p].y);
        const float s0 = bflo(S[p].x), s1 = bfhi(S[p].x), s2 = bflo(S[p].y), s3 = bfhi(S[p].y);
#pragma unroll
        for (int r = 0; r < 2; ++r) {
          const v4u t = Q[2 * p + r];
          const u32 a0 = pkbf((float)(int)t.x, (float)(int)t.y), a1 = pkbf((float)(int)t.z, (float)(int)t.w);
          const u32 c0 = pkbf(bflo(a0) - z0, bfhi(a0) - z1), c1 = pkbf(bflo(a1) - z2, bfhi(a1) - z3);
          W[4 * p + 2 * r] = pkbf(bflo(c0) * s0, bfhi(c0) * s1);
          W[4 * p + 2 * r + 1] = pkbf(bflo(c1) * s2, bfhi(c1) * s3);
          asm volatile("" : "+v"(W[4 * p + 2 * r]), "+v"(W[4 * p + 2 * r + 1]));
        }
      };
#pragma unroll
      for (int p = 0; p < 9; ++p) {
        const int grp = (144 * w + 16 * p) >> 5;
        Z[p] = __builtin_amdgcn_raw_buffer_load_b64(rz, soff, grp * VP, 0);
        S[p] = __builtin_amdgcn_raw_buffer_load_b64(rsn, soff, grp * VP, 0);
#pragma unroll
        for (int r = 0; r < 2; ++r) Q[2 * p + r] = __builtin_amdgcn_raw_buffer_load_b128(rq, qoff, (16 * p + 2 * r) * QP, AUXQ);
        pin();
        if (p >= QLAG && p < 8) { deq(p - QLAG); pin(); }
        if (p == QLAG) {
#pragma unroll
          for (int k = 0; k < UPF; ++k) asm volatile("" :: "v"(pf[k]));
        }
      }
      v4u g[9];
      const u32 goff = WS_XR + ((36 * w + q) * 4 + b) * 16;
#pragma unroll
      for (int p = 8 - QLAG; p <= SPECP; ++p) { deq(p); pin(); }
#pragma unroll
      for (int p = 0; p < 9; ++p) g[p] = ldg4(rw, goff + p * 256);
      pin();
      if (lane == 0) __hip_atomic_fetch_add(&L.issued, 1u, __ATOMIC_RELAXED, __HIP_MEMORY_SCOPE_WORKGROUP);
      STAMP(1);
#pragma unroll
      for (int p = (SPECP + 1 > 8 - QLAG ? SPECP + 1 : 8 - QLAG); p < 9; ++p) { deq(p); pin(); }
      const Sel sel = mksel(lane);
      const u32 tag = wait_flag(&L.tagv);
      STAMP(2);
      fix_granules<9>(g, rw, goff, 256, tag);
      STAMP(4);
      v4f acc0 = {0, 0, 0, 0}, acc1 = acc0;
#pragma unroll
      for (int p = 0; p < 9; ++p) {
        const v4u bw = {W[4 * p], W[4 * p + 1], W[4 * p + 2], W[4 * p + 3]};
        const v4u lo = {perm(g[p].x, sel.aLo), perm(g[p].x, sel.bLo), perm(g[p].z, sel.aLo), perm(g[p].z, sel.bLo)};
        const v4u hi = {perm(g[p].x, sel.aHi), perm(g[p].x, sel.bHi), perm(g[p].z, sel.aHi), perm(g[p].z, sel.bHi)};
        acc0 = mfma32(lo, bw, acc0);
        acc1 = mfma32(hi, bw, acc1);
      }
      {
        float* dst = &L.redC[w][4 * n + q];
#pragma unroll
        for (int r = 0; r < 4; ++r) {
          const float t = acc0[r] + bitsf(__builtin_amdgcn_update_dpp(0, fbits(acc1[r]), 0x128, 0xf, 0xf, false));
          if (n < 8) dst[r * 32] = t;
        }
      }
      if (arrive_last(&L.cnt[0], 4, lane)) {
        if (lane < 32) {
          const float* src = &L.redC[0][b * 32 + (lane & 28)];
          const v4f s = (*(const v4f*)src + *(const v4f*)(src + 256)) + (*(const v4f*)(src + 512) + *(const v4f*)(src + 768));
          stg4(rw, WS_H + id * 512 + lane * 16, v4u{pkbf(s[0], s[1]), tag, pkbf(s[2], s[3]), tag});
        }
        STAMP(5);
        watch_h<8>(L, rw, tag, 16 * (id & 3), 16, true, lane);
        STAMP(6);
      }
    }
  } else if (w == 0) {
    __builtin_amdgcn_s_setprio(3);
    ARGS();
    u32 E = __builtin_amdgcn_raw_buffer_load_b32(rw, WS_E, 0, SC1);
    const u32 tag = __builtin_amdgcn_readfirstlane(E) + 1;
    if (lane == 0) __builtin_amdgcn_raw_buffer_store_b64(v2u{tag, tag}, rw, WS_ACK + wg * 8, 0, SC1);
    STAMP(1);
    if (role == 5) {
#pragma nounroll
      for (int it = 0; it < SPIN_MEM; ++it) {
        cbar();
        const u32 o = fresh(WS_ACK + lane * 16);
        const v4u a0 = ldg4(rw, o), a1 = ldg4(rw, o + 1024);
        const bool ok = a0.x == tag && a0.y == tag && a0.z == tag && a0.w == tag && a1.x == tag && a1.y == tag && a1.z == tag && a1.w == tag;
        if (__all(ok)) break;
        __builtin_amdgcn_s_sleep(8);
      }
      cbar();
      if (lane == 0) __builtin_amdgcn_raw_buffer_store_b32(tag, rw, WS_E, 0, SC1);
      STAMP(2);
    } else {
      watch_h<12>(L, rw, tag, role == 4 ? 88 : role == 2 ? 64 : 80, role == 4 ? 56 : role == 2 ? 24 : 8, role != 4, lane);
      STAMP(2);
      if (role == 4) {
#pragma nounroll
        for (int it = 0; it < SPIN_LDS; ++it) {
          if (__builtin_amdgcn_readfirstlane(__hip_atomic_load(&L.nearEnd, __ATOMIC_RELAXED, __HIP_MEMORY_SCOPE_WORKGROUP))) break;
          __builtin_amdgcn_s_sleep(2);
        }
        cbar();
        STAMP(3);
        const u32 po = WS_P + id * 12288 + lane * 16;
        u32 todo = id < 18 ? 63 : 31;
        v4u g[12];
        g[10] = v4u{0, 0, 0, 0}; g[11] = g[10];
#pragma nounroll
        for (int it = 0; it < SPIN_MEM; ++it) {
          const u32 o = fresh(po);
#pragma unroll
          for (int m = 0; m < 6; ++m)
            if (todo & (1u << m)) { g[2 * m] = ldg4s(rw, o, m * 2048); g[2 * m + 1] = ldg4s(rw, o, m * 2048 + 1024); }
          waitvm<0>();
#pragma unroll
          for (int m = 0; m < 6; ++m)
            if ((todo & (1u << m)) && __all(tagok(g[2 * m], tag) && tagok(g[2 * m + 1], tag))) todo &= ~(1u << m);
          if (!todo) break;
        }
        STAMP(4);
        v4f s = {0, 0, 0, 0};
#pragma unroll
        for (int m = 0; m < 6; ++m) s += v4f{bitsf(g[2 * m].x), bitsf(g[2 * m].z), bitsf(g[2 * m + 1].x), bitsf(g[2 * m + 1].z)};
        *(v4f*)&L.redV[0][(lane & 3) * 64 + 4 * (lane >> 2)] = s;
        if (arrive_last(&L.cnt[1], 8, lane)) { finish(L, OUTP(), id * 64, lane); STAMP(6); }
      }
    }
  } else if (XTRA && role == 4 && w == 7 && (id & 7) == 3) {
    ARGS();
    *(v4f*)&L.redV[7][lane * 4] = v4f{0, 0, 0, 0};
    if (arrive_last(&L.cnt[1], 8, lane)) finish(L, OUTP(), id * 64, lane);
  } else if (role == 4 || (XTRA && w == 1 && (role == 2 ? (id & 3) == 1 : role != 5 && id < 2))) {
    ARGS();
    const bool fi = role == 4, a6 = role == 2;
    v128<KF, PF>(L, VRS(), rw, OUTP(), fi ? id * 64 : (a6 ? id : 4 * id + 1) * 128 + 64, fi ? 2816 + (w - 1) * 256 : 4352, w, lane,
                 a6 ? 0 : id + 1, a6 ? 2 : 4 + 2 * id, fi ? 0 : a6 ? 6 : 2, a6 ? 4 : 5 STPASS);
  } else if (role != 5 && w >= 2) {
    ARGS();
    const int vw = w - 2;
    const bool a6 = role == 2;
    v256<KA, PN, K0, S0, false>(L, VRS(), rw, a6 ? id : 3 * id + (vw >> 1), (a6 ? 16 + vw : 20 + (vw & 1)) * 128, a6 ? vw : vw & 1, w, lane, false,
                 a6 ? 0 : vw >> 1, a6 ? 2 : 2 + (vw & 6), a6 ? 6 : 2,
                 XTRA && (a6 ? (id & 3) == 1 : id < 2 && (vw >> 1) == id + 1), a6 ? 4 : 5 STPASS);
  } else {
    if (w == 1) {
      const rsrc_t rc = mkrs((const void*)(pc0 & ~255ull), CODE_BYTES);
      u32 acc = 0;
#pragma unroll
      for (int i = 0; i < (CODE_BYTES + 8191) / 8192; ++i) acc += __builtin_amdgcn_raw_buffer_load_b32(rc, lane * 128 + i * 8192, 0, 0);
      asm volatile("" :: "v"(acc));
    }
  }
}

}

extern "C" void kernel_launch(void* const* d_in, const int* in_sizes, int n_in, void* d_out, int out_size, void* d_ws,
                              size_t ws_size, hipStream_t stream) {
  qk::quip_kernel<<<256, 512, 0, stream>>>((const unsigned short*)d_in[0], (const int*)d_in[1], (const unsigned short*)d_in[2],
                                          (const unsigned short*)d_in[3], (const unsigned short*)d_in[4],
                                          (const unsigned short*)d_in[5], (unsigned short*)d_out, (unsigned char*)d_ws);
}
```
